```python
import torch

def _softmax(x, axis=-1):
    return torch.softmax(x, dim=axis)
def _gelu(x):
    return torch.nn.functional.gelu(x, approximate="tanh")
def _silu(x):
    return torch.nn.functional.silu(x)
def _sigmoid(x):
    return torch.sigmoid(x)
def _layernorm(x, axis=-1, eps=1e-5):
    m = x.mean(dim=axis, keepdim=True)
    v = x.var(dim=axis, keepdim=True, unbiased=False)
    return (x - m) / torch.sqrt(v + eps)
def _scatter_add(tgt, idx, src, axis=0):
    return tgt.clone().index_add_(axis, idx, src)
def _segment_sum(src, seg, n):
    out = torch.zeros((n,) + src.shape[1:], dtype=src.dtype)
    return out.index_add_(0, seg, src)
def _pad(x, pw):
    flat = tuple(v for pair in reversed(pw) for v in pair)
    return torch.nn.functional.pad(x, flat)

kernel_name = 'sequential_preLN__sliding_local_dilated__dense_activation_gelu__prompt'

def setup_inputs():

    _w = (lambda a: (lambda t: t.to(torch.bfloat16) if t.is_floating_point() else t)(torch.as_tensor(a)))
    def _A_setup():
        B, S, D = 1, 2048, 640
        return {
            "x": _w((lambda *s: torch.randn(*s, dtype=torch.bfloat16) * 0.05)(B, S, D)),
            "wqkv": _w((lambda *s: torch.randn(*s, dtype=torch.bfloat16) * 0.05)(D, 3 * D)),
            "wo": _w((lambda *s: torch.randn(*s, dtype=torch.bfloat16) * 0.05)(D, D)),
        }
    _A_in = _A_setup()

    def _B_setup():
        B, S, D, F = 1, 2048, 640, 1728
        return {
            "x": _w((lambda *s: torch.randn(*s, dtype=torch.bfloat16) * 0.05)(B, S, D)),
            "w1": _w((lambda *s: torch.randn(*s, dtype=torch.bfloat16) * 0.8)(D, F) * (D ** -0.5)),
            "b1": _w((lambda *s: torch.randn(*s, dtype=torch.bfloat16) * 0.8)(F) * 0.01),
            "w2": _w((lambda *s: torch.randn(*s, dtype=torch.bfloat16) * 0.8)(F, D) * (F ** -0.5)),
            "b2": _w((lambda *s: torch.randn(*s, dtype=torch.bfloat16) * 0.8)(D) * 0.01),
        }
    _B_in = _B_setup()

    return {
        "A_x": _A_in["x"],
        "A_wqkv": _A_in["wqkv"],
        "A_wo": _A_in["wo"],
        "B_x": _B_in["x"],
        "B_w1": _B_in["w1"],
        "B_b1": _B_in["b1"],
        "B_w2": _B_in["w2"],
        "B_b2": _B_in["b2"],
    }

def reference(A_x, A_wqkv, A_wo, B_x, B_w1, B_b1, B_w2, B_b2):
    def _A_ref(x, wqkv, wo):
        B, S, D = x.shape
        H = 32; Dh = D // H
        W, dil = 8, 2
        qkv = (lambda x, *d: x.permute(*d))((x @ wqkv).reshape(B, S, 3, H, Dh), 2, 0, 3, 1, 4)
        q, k, v = qkv[0], qkv[1], qkv[2]
        scale = Dh ** -0.5
        pos = torch.arange(S)
        off = torch.arange(W) * dil
        idx = pos[:, None] - off[None, :]
        mask = idx >= 0
        idx_c = torch.where(mask, idx, 0).reshape(-1)
        kg = k[:, :, idx_c, :].reshape(B, H, S, W, Dh)
        vg = v[:, :, idx_c, :].reshape(B, H, S, W, Dh)
        logits = torch.einsum("bhsd,bhswd->bhsw", q, kg) * scale
        logits = torch.where(mask[None, None, :, :], logits, -1e9)
        attn = _softmax(logits, axis=-1)
        o = torch.einsum("bhsw,bhswd->bhsd", attn, vg)
        return (lambda x, *d: x.permute(*d))(o, 0, 2, 1, 3).reshape(B, S, D) @ wo

    def _B_ref(x, w1, b1, w2, b2):
        h = torch.einsum("bsd,df->bsf", x, w1) + b1
        h = _gelu(h)
        return torch.einsum("bsf,fd->bsd", h, w2) + b2

    x = A_x
    h = x + _A_ref(_layernorm(x, axis=-1), A_wqkv, A_wo)
    h2 = h + _B_ref(_layernorm(h, axis=-1), B_w1, B_b1, B_w2, B_b2)
    return h2
```

```cpp
#include <hip/hip_runtime.h>
#include <hip/hip_bf16.h>
#include <stdint.h>

typedef unsigned short u16;
typedef short bf16x8 __attribute__((ext_vector_type(8)));
typedef float f32x4 __attribute__((ext_vector_type(4)));

__device__ __forceinline__ float bf2f(u16 b){ return __builtin_bit_cast(float, (unsigned)b<<16); }
__device__ __forceinline__ u16 f2bf(float f){ unsigned u=__builtin_bit_cast(unsigned,f); u += 0x7FFFu + ((u>>16)&1u); return (u16)(u>>16); }

constexpr int S=2048, D=640, F=1728, H=32, DH=20;

__global__ void ln_k(const u16* x, u16* y){
  int row=blockIdx.x; int t=threadIdx.x;
  float v[10]; float s=0;
  for(int i=0;i<10;i++){ v[i]=bf2f(x[row*D+t+64*i]); s+=v[i]; }
  for(int o=32;o;o>>=1) s+=__shfl_xor(s,o);
  float m=s/D; float q=0;
  for(int i=0;i<10;i++){ float d=v[i]-m; q+=d*d; }
  for(int o=32;o;o>>=1) q+=__shfl_xor(q,o);
  float r=rsqrtf(q/D+1e-5f);
  for(int i=0;i<10;i++) y[row*D+t+64*i]=f2bf((v[i]-m)*r);
}

template<int MODE>
__global__ void gemm_k(const u16* A, const u16* B, u16* C, const u16* bias, const u16* res, int M, int N, int K){
  int lane=threadIdx.x&63; int w=threadIdx.x>>6;
  int tn=blockIdx.x*4+w, tm=blockIdx.y;
  if(tn*16>=N) return;
  f32x4 acc={0,0,0,0};
  for(int k=0;k<K;k+=32){
    bf16x8 a=*(const bf16x8*)(A+(size_t)(tm*16+(lane&15))*K+k+8*(lane>>4));
    bf16x8 b;
    for(int j=0;j<8;j++) b[j]=(short)B[(size_t)(k+8*(lane>>4)+j)*N+tn*16+(lane&15)];
    acc=__builtin_amdgcn_mfma_f32_16x16x32_bf16(a,b,acc,0,0,0);
  }
  int col=tn*16+(lane&15);
  for(int j=0;j<4;j++){
    int row=tm*16+(lane>>4)*4+j;
    float v=acc[j];
    if(MODE==2||MODE==3) v+=bf2f(bias[col]);
    if(MODE==2){ float u=0.7978845608028654f*(v+0.044715f*v*v*v); v=0.5f*v*(1.f+tanhf(u)); }
    if(MODE==1||MODE==3) v+=bf2f(res[(size_t)row*N+col]);
    C[(size_t)row*N+col]=f2bf(v);
  }
}

__global__ void attn_k(const u16* qkv, u16* o){
  int idx=blockIdx.x*blockDim.x+threadIdx.x;
  int s=idx>>5, h=idx&31;
  float q[DH];
  for(int d=0;d<DH;d++) q[d]=bf2f(qkv[(size_t)s*3*D+h*DH+d]);
  float l[8]; float mx=-1e30f;
  for(int w=0;w<8;w++){
    int t=s-2*w; float a=0;
    if(t>=0){ for(int d=0;d<DH;d++) a+=q[d]*bf2f(qkv[(size_t)t*3*D+D+h*DH+d]); a*=0.22360679774997896f; }
    else a=-1e9f;
    l[w]=a; mx=fmaxf(mx,a);
  }
  float sum=0; for(int w=0;w<8;w++){ l[w]=__expf(l[w]-mx); sum+=l[w]; }
  float inv=1.f/sum;
  float acc[DH]; for(int d=0;d<DH;d++) acc[d]=0;
  for(int w=0;w<8;w++){ int t=s-2*w; if(t<0) t=0; float p=l[w]*inv;
    for(int d=0;d<DH;d++) acc[d]+=p*bf2f(qkv[(size_t)t*3*D+2*D+h*DH+d]); }
  for(int d=0;d<DH;d++) o[(size_t)s*D+h*DH+d]=f2bf(acc[d]);
}

extern "C" void kernel_launch(void* const* d_in, const int* in_sizes, int n_in,
                              void* d_out, int out_size, void* d_ws, size_t ws_size,
                              hipStream_t stream) {
  const u16* x=(const u16*)d_in[0]; const u16* wqkv=(const u16*)d_in[1]; const u16* wo=(const u16*)d_in[2];
  const u16* w1=(const u16*)d_in[4]; const u16* b1=(const u16*)d_in[5]; const u16* w2=(const u16*)d_in[6]; const u16* b2=(const u16*)d_in[7];
  u16* out=(u16*)d_out;
  u16* ws=(u16*)d_ws;
  u16* ln=ws; u16* qkv=ln+S*D; u16* o=qkv+S*3*D; u16* h=o+S*D; u16* g=h+S*D;
  ln_k<<<S,64,0,stream>>>(x,ln);
  gemm_k<0><<<dim3(3*D/64,S/16),256,0,stream>>>(ln,wqkv,qkv,nullptr,nullptr,S,3*D,D);
  attn_k<<<S*H/256,256,0,stream>>>(qkv,o);
  gemm_k<1><<<dim3(D/64,S/16),256,0,stream>>>(o,wo,h,nullptr,x,S,D,D);
  ln_k<<<S,64,0,stream>>>(h,ln);
  gemm_k<2><<<dim3(F/64,S/16),256,0,stream>>>(ln,w1,g,b1,nullptr,S,F,D);
  gemm_k<3><<<dim3(D/64,S/16),256,0,stream>>>(g,w2,out,b2,h,S,D,F);
}
```

```cpp
#include <hip/hip_runtime.h>
#include <hip/hip_bf16.h>
#include <stdint.h>

typedef unsigned short u16;
typedef short bf16x8 __attribute__((ext_vector_type(8)));
typedef float f32x4 __attribute__((ext_vector_type(4)));

__device__ __forceinline__ float bf2f(u16 b){ return __builtin_bit_cast(float, (unsigned)b<<16); }
__device__ __forceinline__ u16 f2bf(float f){ unsigned u=__builtin_bit_cast(unsigned,f); u += 0x7FFFu + ((u>>16)&1u); return (u16)(u>>16); }

constexpr int S=2048, D=640, F=1728, H=32, DH=20;

__global__ void ln_k(const u16* x, u16* y){
  int row=blockIdx.x; int t=threadIdx.x;
  float v[10]; float s=0;
  for(int i=0;i<10;i++){ v[i]=bf2f(x[row*D+t+64*i]); s+=v[i]; }
  for(int o=32;o;o>>=1) s+=__shfl_xor(s,o);
  float m=s/D; float q=0;
  for(int i=0;i<10;i++){ float d=v[i]-m; q+=d*d; }
  for(int o=32;o;o>>=1) q+=__shfl_xor(q,o);
  float r=rsqrtf(q/D+1e-5f);
  for(int i=0;i<10;i++) y[row*D+t+64*i]=f2bf((v[i]-m)*r);
}

template<int MODE>
__global__ void gemm_k(const u16* A, const u16* B, u16* C, const u16* bias, const u16* res, int M, int N, int K){
  int lane=threadIdx.x&63; int w=threadIdx.x>>6;
  int tn=blockIdx.x*4+w, tm=blockIdx.y;
  if(tn*16>=N) return;
  f32x4 acc={0,0,0,0};
  for(int k=0;k<K;k+=32){
    bf16x8 a=*(const bf16x8*)(A+(size_t)(tm*16+(lane&15))*K+k+8*(lane>>4));
    bf16x8 b;
    for(int j=0;j<8;j++) b[j]=(short)B[(size_t)(k+8*(lane>>4)+j)*N+tn*16+(lane&15)];
    acc=__builtin_amdgcn_mfma_f32_16x16x32_bf16(a,b,acc,0,0,0);
  }
  int col=tn*16+(lane&15);
  for(int j=0;j<4;j++){
    int row=tm*16+(lane>>4)*4+j;
    float v=acc[j];
    if(MODE==2||MODE==3) v+=bf2f(bias[col]);
    if(MODE==2){ float u=0.7978845608028654f*(v+0.044715f*v*v*v); v=0.5f*v*(1.f+tanhf(u)); }
    if(MODE==1||MODE==3) v+=bf2f(res[(size_t)row*N+col]);
    C[(size_t)row*N+col]=f2bf(v);
  }
}

__global__ void attn_k(const u16* qkv, u16* o){
  int idx=blockIdx.x*blockDim.x+threadIdx.x;
  int s=idx>>5, h=idx&31;
  float q[DH];
  for(int d=0;d<DH;d++) q[d]=bf2f(qkv[(size_t)s*3*D+h*DH+d]);
  float l[8]; float mx=-1e30f;
  for(int w=0;w<8;w++){
    int t=s-2*w; float a=0;
    if(t>=0){ for(int d=0;d<DH;d++) a+=q[d]*bf2f(qkv[(size_t)t*3*D+D+h*DH+d]); a*=0.22360679774997896f; }
    else a=-1e9f;
    l[w]=a; mx=fmaxf(mx,a);
  }
  float sum=0; for(int w=0;w<8;w++){ l[w]=__expf(l[w]-mx); sum+=l[w]; }
  float inv=1.f/sum;
  float acc[DH]; for(int d=0;d<DH;d++) acc[d]=0;
  for(int w=0;w<8;w++){ int t=s-2*w; if(t<0) t=0; float p=l[w]*inv;
    for(int d=0;d<DH;d++) acc[d]+=p*bf2f(qkv[(size_t)t*3*D+2*D+h*DH+d]); }
  for(int d=0;d<DH;d++) o[(size_t)s*D+h*DH+d]=f2bf(acc[d]);
}


typedef unsigned v4u __attribute__((ext_vector_type(4)));
template<int MODE>
__global__ __launch_bounds__(256) void meas_k(const char* a, const char* b, const char* c, const char* d, const char* e, unsigned* sink, int amp){
  unsigned long long t0=__builtin_amdgcn_s_memrealtime();
  __builtin_amdgcn_s_waitcnt(0xC07F);
  asm volatile("" :: "s"(a), "s"(b), "s"(amp));
  v4u acc={0,0,0,0};
  unsigned long long ta=__builtin_amdgcn_s_memrealtime();
  __builtin_amdgcn_s_waitcnt(0xC07F);
  if(MODE==1){
    int t=threadIdx.x; size_t o=(size_t)blockIdx.x*8192+t*16;
    v4u v[10];
    v[0]=*(const v4u*)(a+o); v[1]=*(const v4u*)(a+o+4096);
    v[2]=*(const v4u*)(b+o); v[3]=*(const v4u*)(b+o+4096);
    v[4]=*(const v4u*)(c+o); v[5]=*(const v4u*)(c+o+4096);
    v[6]=*(const v4u*)(d+o); v[7]=*(const v4u*)(d+o+4096);
    v[8]=*(const v4u*)(e+o); v[9]=*(const v4u*)(e+o+4096);
    __builtin_amdgcn_sched_barrier(0);
    for(int i=0;i<10;i++) acc^=v[i];
    asm volatile("" : "+v"(acc));
  }
  __builtin_amdgcn_sched_barrier(0);
  unsigned long long t1=__builtin_amdgcn_s_memrealtime();
  __builtin_amdgcn_s_waitcnt(0xC07F);
  unsigned long long dt = MODE==0 ? (ta-t0) : (t1-ta);
  unsigned long long until=t1+dt*amp;
  while(__builtin_amdgcn_s_memrealtime()<until) __builtin_amdgcn_s_sleep(1);
  if((acc.x^acc.y^acc.z^acc.w)==0x12345678u) sink[0]=1;
}

extern "C" void kernel_launch(void* const* d_in, const int* in_sizes, int n_in,
                              void* d_out, int out_size, void* d_ws, size_t ws_size,
                              hipStream_t stream) {
  const u16* x=(const u16*)d_in[0]; const u16* wqkv=(const u16*)d_in[1]; const u16* wo=(const u16*)d_in[2];
  const u16* w1=(const u16*)d_in[4]; const u16* b1=(const u16*)d_in[5]; const u16* w2=(const u16*)d_in[6]; const u16* b2=(const u16*)d_in[7];
  u16* out=(u16*)d_out;
  u16* ws=(u16*)d_ws;
  u16* ln=ws; u16* qkv=ln+S*D; u16* o=qkv+S*3*D; u16* h=o+S*D; u16* g=h+S*D;
  meas_k<1><<<256,256,0,stream>>>((const char*)d_in[0],(const char*)d_in[1],(const char*)d_in[3],(const char*)d_in[4],(const char*)d_in[6],(unsigned*)((char*)d_ws+(200<<20)),0);
  ln_k<<<S,64,0,stream>>>(x,ln);
  gemm_k<0><<<dim3(3*D/64,S/16),256,0,stream>>>(ln,wqkv,qkv,nullptr,nullptr,S,3*D,D);
  attn_k<<<S*H/256,256,0,stream>>>(qkv,o);
  gemm_k<1><<<dim3(D/64,S/16),256,0,stream>>>(o,wo,h,nullptr,x,S,D,D);
  ln_k<<<S,64,0,stream>>>(h,ln);
  gemm_k<2><<<dim3(F/64,S/16),256,0,stream>>>(ln,w1,g,b1,nullptr,S,F,D);
  gemm_k<3><<<dim3(D/64,S/16),256,0,stream>>>(g,w2,out,b2,h,S,D,F);
}
```

```cpp
#include <hip/hip_runtime.h>
#include <stdint.h>

__global__ __launch_bounds__(256, 1) void block_fwd(const void* x, const void* wqkv, const void* wo, const void* w1,
                                                     const void* b1, const void* w2, const void* b2, void* out, void* ws) {
#if defined(__HIP_DEVICE_COMPILE__)
  unsigned tid = threadIdx.x;
  asm volatile(
R"ASM(
s_mov_b64 s[96:97], %1
s_mov_b32 s98, %2
s_load_dwordx16 s[16:31], s[96:97], 0x0
s_load_dwordx2 s[92:93], s[96:97], 0x40
v_lshrrev_b32 v21, 6, v0
v_and_b32 v20, 63, v0
v_cmp_eq_u32_e32 vcc, 0, v0
v_readfirstlane_b32 s34, v21
v_bfrev_b32 v9, 1
v_mov_b32 v2, 0
v_cndmask_b32 v2, v9, v2, vcc
s_and_b32 s43, s98, 7
s_lshr_b32 s39, s98, 3
s_lshr_b32 s44, s43, 1
s_lshl_b32 s44, s44, 3
s_lshr_b32 s45, s39, 2
s_add_u32 s35, s44, s45
s_and_b32 s43, s43, 1
s_lshl_b32 s43, s43, 2
s_and_b32 s45, s39, 3
s_add_u32 s36, s43, s45
s_getpc_b64 s[12:13]
.Lk_pc:
s_mov_b32 s14, .Lk_end-.Lk_pc-4
s_and_b32 s13, s13, 0xffff
s_mov_b32 s15, 0x20000
s_lshl_b32 s43, s39, 2
s_add_u32 s43, s43, s34
s_lshl_b32 s43, s43, 9
v_lshl_add_u32 v26, v20, 7, s43
v_cmp_gt_u32_e32 vcc, 4, v20
s_lshl_b32 s44, s34, 8
s_add_u32 m0, s44, 0x24200
v_cndmask_b32 v26, v9, v26, vcc
buffer_load_dword v26, s[12:15], 0 offen lds
s_lshl_b32 s43, s34, 5
s_add_u32 s60, s35, s43
s_mul_i32 s50, s36, 0xa0
s_mul_i32 s44, s60, 0x7800
s_cmp_lt_u32 s60, 0x50
s_cselect_b32 s61, 0, 0x80000000
s_cselect_b32 s62, s44, 0
v_and_b32 v28, 31, v20
v_mul_u32_u24 v29, 26, v28
v_lshrrev_b32 v29, 8, v29
v_mul_u32_u24 v29, 0x460, v29
v_lshrrev_b32 v30, 5, v20
v_mul_u32_u24 v30, 0xf00, v30
v_lshl_add_u32 v27, v28, 4, s50
v_cmp_gt_u32_e32 vcc, 30, v28
v_add3_u32 v27, v27, v29, v30
v_or_b32 v27, s61, v27
v_cndmask_b32 v27, v9, v27, vcc
v_lshrrev_b32 v24, 3, v20
v_and_b32 v25, 7, v20
s_lshl_b32 s43, s35, 6
s_lshl_b32 s44, s34, 4
s_add_u32 s63, s43, s44
s_and_b32 s45, s34, 1
s_lshl_b32 s45, s45, 3
s_add_u32 s45, s45, s43
s_sub_u32 s45, s45, 16
s_mul_i32 s44, s63, 0x500
s_mul_i32 s45, s45, 0x500
s_cmp_eq_u32 s35, 0
s_cselect_b32 s45, 0x80000000, s45
v_and_b32 v31, 6, v24
v_xor_b32 v31, v31, v25
v_mul_u32_u24 v30, 0x500, v24
v_lshl_add_u32 v30, v31, 4, v30
v_add_u32 v28, s44, v30
v_add_u32 v29, s45, v30
v_add_u32 v26, 0x2800, v28
s_lshr_b32 s64, s34, 1
s_mul_i32 s64, s64, 0x280
s_lshl_b32 s43, s34, 12
s_add_u32 s43, s43, 0x19000
s_cmp_lt_u32 s34, 3
s_cselect_b32 s43, s43, 0x26d00
s_lshl_b32 s65, s36, 7
v_mov_b32 v30, 0x8000
s_mov_b32 m0, s43
s_mov_b32 s14, 0x258000
s_mov_b32 s10, 0x280000
s_mov_b32 s11, 0x20000
s_mov_b32 s6, 0x1430000
s_mov_b32 s7, 0x20000
s_mov_b32 s43, s34
s_and_b32 s43, s43, 1
s_lshl_b32 s43, s43, 10
s_lshl4_add_u32 s75, s64, s43
s_add_u32 s43, s34, 1
s_mul_i32 s56, s43, 0x5000
s_waitcnt lgkmcnt(0)
s_mov_b32 s4, s92
s_and_b32 s5, s93, 0xffff
buffer_load_dwordx2 v[4:5], v30, s[4:7], 0 offen sc1
s_mov_b32 s12, s18
s_and_b32 s13, s19, 0xffff
buffer_load_dwordx4 v27, s[12:15], s62 offen lds
s_add_u32 m0, m0, 0x400
s_add_u32 s62, s62, 0x1e00
buffer_load_dwordx4 v27, s[12:15], s62 offen lds
s_add_u32 m0, m0, 0x400
s_add_u32 s62, s62, 0x1e00
buffer_load_dwordx4 v27, s[12:15], s62 offen lds
s_add_u32 m0, m0, 0x400
s_add_u32 s62, s62, 0x1e00
buffer_load_dwordx4 v27, s[12:15], s62 offen lds
s_mov_b32 s8, s16
s_and_b32 s9, s17, 0xffff
s_add_u32 m0, s75, 0
s_nop 0
buffer_load_dwordx4 v29, s[8:11], s64 offen lds
s_add_u32 m0, s75, 0x800
s_add_u32 s64, s64, 0x80
buffer_load_dwordx4 v29, s[8:11], s64 offen lds
s_add_u32 m0, s75, 0x1000
s_add_u32 s64, s64, 0x80
buffer_load_dwordx4 v29, s[8:11], s64 offen lds
s_add_u32 m0, s75, 0x1800
s_add_u32 s64, s64, 0x80
buffer_load_dwordx4 v29, s[8:11], s64 offen lds
s_add_u32 m0, s75, 0x2000
s_add_u32 s64, s64, 0x80
buffer_load_dwordx4 v29, s[8:11], s64 offen lds
s_add_u32 s76, s56, 0x400
s_lshl4_add_u32 m0, s65, s56
s_nop 0
buffer_load_dwordx4 v28, s[8:11], s65 offen lds
s_lshl4_add_u32 m0, s65, s76
s_nop 0
buffer_load_dwordx4 v26, s[8:11], s65 offen lds
v_lshlrev_b32 v0, 4, v20
v_lshlrev_b32 v1, 3, v20
v_and_b32 v22, 15, v20
v_lshrrev_b32 v23, 4, v20
s_lshr_b32 s37, s35, 1
s_and_b32 s43, s35, 1
s_lshl_b32 s43, s43, 3
s_add_u32 s38, s43, s36
v_and_b32 v31, 3, v20
v_lshrrev_b32 v32, 2, v22
v_mul_u32_u24 v37, 0xa0, v23
v_lshl_add_u32 v37, v31, 3, v37
v_mul_u32_u24 v38, 0xa0, v31
v_add_u32 v39, 32, v38
v_cmp_eq_u32_e32 vcc, 3, v31
v_cmp_eq_u32_e64 s[48:49], 3, v23
s_nop 0
v_cndmask_b32 v38, v39, v38, vcc
s_and_b64 vcc, vcc, s[48:49]
v_cndmask_b32_e64 v37, v37, v38, s[48:49]
v_mov_b32 v39, 40
s_lshl_b32 s43, s34, 12
s_cmp_lt_u32 s34, 3
s_cselect_b32 s43, s43, 0
s_add_u32 s43, s43, 0x19000
v_cndmask_b32_e64 v39, v39, 0, vcc
v_lshl_add_u32 v37, v32, 9, v37
v_add_u32 v33, s43, v37
v_add_u32 v34, v33, v39
v_add_u32 v35, v34, v39
v_add_u32 v36, v35, v39
v_lshlrev_b32 v228, 8, v32
v_lshl_add_u32 v228, v31, 3, v228
s_and_b32 s44, s60, 3
s_lshl_b32 s45, s44, 8
s_mul_i32 s44, s44, 0xc0
v_lshlrev_b32 v31, 4, v22
v_lshl_or_b32 v31, v23, 10, v31
v_cmp_le_u32_e32 vcc, 60, v20
v_add_u32 v38, s44, v31
v_add_u32 v31, s45, v31
v_cndmask_b32 v38, v38, v9, vcc
v_cndmask_b32_e64 v31, v31, v38, s[48:49]
v_or_b32 v31, s61, v31
v_mul_u32_u24 v161, 0xc0, v23
v_cmp_gt_u32_e32 vcc, 12, v22
v_lshl_add_u32 v161, v22, 4, v161
s_mul_i32 s43, s36, 0x50
s_lshr_b32 s44, s60, 2
s_add_u32 s43, s43, s44
s_mul_i32 s43, s43, 0xf00
v_cndmask_b32 v161, v9, v161, vcc
s_add_u32 s43, s43, 0x40000
s_cmp_lt_u32 s60, 0x50
s_cselect_b32 s62, s43, 0
s_mov_b32 s91, 0x3f803f80
s_mov_b32 s57, 0x3acccccd
s_mov_b32 s58, 0x3727c5ac
s_mov_b32 s59, 0xffff0000
s_lshr_b32 s43, s35, 3
s_lshl_b32 s43, s43, 7
s_and_b32 s44, s35, 7
s_lshl_b32 s44, s44, 3
s_add_u32 s43, s43, s44
s_lshl_b32 s44, s36, 9
s_add_u32 s55, s43, s44
v_and_b32 v3, 24, v20
)ASM"
R"ASM(
v_lshlrev_b32 v3, 4, v3
v_lshl_add_u32 v3, v25, 3, v3
v_add_u32 v3, s44, v3
s_waitcnt vmcnt(7)
ds_read_b64_tr_b16 v[140:141], v33
ds_read_b64_tr_b16 v[142:143], v33 offset:2048
ds_read_b64_tr_b16 v[144:145], v34
ds_read_b64_tr_b16 v[146:147], v34 offset:2048
ds_read_b64_tr_b16 v[148:149], v35
ds_read_b64_tr_b16 v[150:151], v35 offset:2048
ds_read_b64_tr_b16 v[152:153], v36
ds_read_b64_tr_b16 v[154:155], v36 offset:2048
v_readfirstlane_b32 s40, v4
v_readfirstlane_b32 s41, v5
s_nop 0
s_add_u32 s40, s40, 1
s_addc_u32 s41, s41, 0
s_waitcnt lgkmcnt(6)
buffer_store_dwordx4 v[140:143], v31, s[4:7], s62 offen sc1
s_add_u32 s62, s62, 0x12c00
s_waitcnt lgkmcnt(4)
buffer_store_dwordx4 v[144:147], v31, s[4:7], s62 offen sc1
s_add_u32 s62, s62, 0x12c00
s_waitcnt lgkmcnt(2)
buffer_store_dwordx4 v[148:151], v31, s[4:7], s62 offen sc1
s_add_u32 s62, s62, 0x12c00
s_waitcnt lgkmcnt(0)
buffer_store_dwordx4 v[152:155], v31, s[4:7], s62 offen sc1
v_mov_b32 v4, s40
v_mov_b32 v5, s41
v_mov_b32 v184, 0
v_mov_b32 v185, 0
v_mov_b32 v202, 0
v_mov_b32 v203, 0
v_mov_b32 v204, 0
v_mov_b32 v205, 0
v_mov_b32 v76, s91
v_mov_b32 v77, s91
v_mov_b32 v78, s91
v_mov_b32 v79, s91
s_lshl_b32 s43, s34, 6
s_add_u32 s43, s43, 0x24000
v_lshl_add_u32 v211, v24, 3, s43
s_add_u32 s43, s43, 0x240
v_lshl_add_u32 v212, v24, 2, s43
v_lshlrev_b32 v213, 11, v24
v_lshl_add_u32 v213, v25, 4, v213
v_add_u32 v213, s75, v213
s_and_b32 s43, s34, 1
s_cmp_eq_u32 s43, 0
s_cselect_b32 s43, 40, 0
v_cmp_gt_u32_e32 vcc, s43, v20
v_mov_b32 v210, 0x24600
s_lshl_b32 s43, s36, 2
s_add_u32 s43, s43, s34
s_mul_i32 s43, s43, 0x12c00
s_add_u32 s39, s43, 0x40000
v_cndmask_b32 v213, v210, v213, vcc
v_lshlrev_b32 v210, 3, v22
v_add_u32 v210, 0x24000, v210
s_mul_i32 s43, s38, 7
s_mul_i32 s44, s38, 6
s_add_u32 s44, s44, 12
s_min_u32 s51, s43, s44
s_cmp_lt_u32 s35, 8
s_cselect_b32 s43, 2, 3
s_cmp_ge_u32 s34, s43
s_cselect_b32 s87, 1, 0
s_sub_u32 s43, s35, 16
s_cmp_lt_u32 s43, 10
s_cselect_b32 s43, 3, 2
s_cmp_lt_u32 s34, s43
s_cselect_b32 s47, 1, 0
s_or_b32 s88, s47, s87
s_add_u32 s43, s35, 48
s_cmp_lt_u32 s34, 2
s_cselect_b32 s19, s60, s43
s_cmp_lt_u32 s19, 20
s_cselect_b32 s85, s47, 0
s_cmp_eq_u32 s34, 3
s_cselect_b32 s43, 0, 16
s_add_u32 s86, s37, s43
s_mul_i32 s43, s86, 52
s_lshr_b32 s43, s43, 8
s_mul_i32 s44, s43, 5
s_sub_u32 s44, s86, s44
s_mul_i32 s43, s43, 0xa0
s_lshl_b32 s45, s44, 5
s_cmp_ge_u32 s44, 2
s_cselect_b32 s46, 16, 0
s_add_u32 s45, s45, s46
s_cmp_eq_u32 s44, 4
s_cselect_b32 s45, 64, s45
s_cselect_b32 s46, 0x50, 16
s_add_u32 s80, s43, s45
s_add_u32 s81, s80, s46
s_sub_u32 s89, s19, 20
s_mul_i32 s43, s89, 37
s_lshr_b32 s43, s43, 9
s_mul_i32 s44, s43, 14
s_sub_u32 s44, s89, s44
s_lshl_b32 s45, s44, 1
s_sub_u32 s45, s45, 24
s_sub_u32 s46, s44, 8
s_cmp_lt_u32 s44, 12
s_cselect_b32 s45, s46, s45
s_cselect_b32 s47, 4, 6
s_cselect_b32 s78, 1, 7
s_lshr_b32 s46, s44, 1
s_and_b32 s82, s44, 1
s_lshl_b32 s82, s82, 1
s_cmp_lt_u32 s44, 8
s_cselect_b32 s45, s46, s45
s_cselect_b32 s47, s82, s47
s_lshl_b32 s43, s43, 2
s_add_u32 s43, s43, s45
s_mul_i32 s44, s43, 7
s_mul_i32 s45, s43, 6
s_add_u32 s45, s45, 12
s_min_u32 s44, s44, s45
s_add_u32 s44, s44, s47
s_add_u32 s45, s44, s78
s_lshl_b32 s44, s44, 4
s_lshl_b32 s45, s45, 4
s_lshl_b32 s46, s19, 5
s_add_u32 s47, s46, 16
s_mul_i32 s52, s36, 0x19000
s_add_u32 s52, s52, 0x2c0000
s_mul_i32 s53, s36, 0x46000
s_add_u32 s53, s53, 0x5b8000
s_mul_i32 s43, s19, 0x1400
s_add_u32 s43, s43, s52
s_mul_i32 s77, s89, 0x1400
s_add_u32 s77, s77, s53
s_cmp_lg_u32 s85, 0
s_cselect_b32 s44, s46, s44
s_cselect_b32 s45, s47, s45
s_cselect_b32 s77, s43, s77
s_cselect_b64 s[16:17], s[20:21], s[26:27]
s_mov_b32 s18, 0x21c000
s_cselect_b32 s18, 0xc8000, s18
s_cselect_b32 s43, 11, 10
s_mov_b32 s46, 0x1000
s_cselect_b32 s46, 0x400, s46
s_lshl_b32 s90, s34, 13
s_add_u32 s90, s90, 0x1c000
v_lshlrev_b32 v229, s43, v23
v_add3_u32 v228, v228, v229, s90
v_add_u32 v229, s46, v228
s_mul_i32 s54, s38, 0x23000
s_add_u32 s54, s54, 0x388000
s_add_u32 s43, s86, 0x78
s_lshl_b32 s43, s43, 10
s_add_u32 s43, s43, s54
s_lshl_b32 s46, s51, 5
s_cmp_lt_u32 s38, 12
s_cselect_b32 s47, 14, 12
s_cmp_lg_u32 s87, 0
s_cselect_b32 s80, s80, s44
s_cselect_b32 s81, s81, s45
s_cselect_b32 s77, s43, s77
s_cselect_b64 s[16:17], s[22:23], s[16:17]
s_cselect_b32 s18, 0x21c000, s18
s_mov_b32 s79, 0x500
s_cselect_b32 s79, 0xd80, s79
s_mov_b32 s82, 0x400
s_cselect_b32 s82, 0xffffb000, s82
s_cselect_b32 s84, 0, 0x80000000
s_cselect_b32 s46, s46, s50
s_cselect_b32 s47, s47, 10
s_mul_i32 s80, s80, s79
s_mul_i32 s81, s81, s79
s_cmp_lg_u32 s88, 0
s_cselect_b32 s80, s80, 0
s_cselect_b32 s81, s81, 0
s_cselect_b32 s77, s77, 0
s_cselect_b32 s83, 0, 0x80000000
s_cselect_b32 s47, s47, 0
s_mul_i32 s78, s38, 20
s_add_u32 s78, s78, s86
s_lshl_b32 s78, s78, 9
s_add_u32 s78, s78, 0x7e8000
v_mul_u32_u24 v38, s79, v23
v_lshlrev_b32 v37, 1, v23
v_xor_b32 v37, v37, v22
v_add_u32 v38, s46, v38
v_cmp_gt_u32_e32 vcc, s47, v37
v_lshl_add_u32 v30, v37, 4, v38
v_xor_b32 v37, 8, v37
v_cndmask_b32 v30, v9, v30, vcc
v_cmp_gt_u32_e32 vcc, s47, v37
v_lshl_add_u32 v27, v37, 4, v38
v_or_b32 v230, s83, v0
v_cndmask_b32 v27, v9, v27, vcc
v_lshrrev_b32 v37, 3, v228
v_and_b32 v37, 0xe0, v37
v_or_b32 v228, v228, v37
v_lshrrev_b32 v37, 3, v229
v_and_b32 v37, 0xe0, v37
v_or_b32 v229, v229, v37
)ASM"
R"ASM(
v_or_b32 v231, s84, v0
v_mov_b32 v233, v22
s_add_u32 s66, s65, 0x80
s_cmp_eq_u32 s66, 0x500
s_cselect_b32 s66, 0, s66
s_lshl4_add_u32 m0, s66, s56
s_nop 0
buffer_load_dwordx4 v28, s[8:11], s66 offen lds
s_lshl4_add_u32 m0, s66, s76
s_nop 0
buffer_load_dwordx4 v26, s[8:11], s66 offen lds
v_lshlrev_b32 v232, 4, v23
v_lshl_or_b32 v232, v22, 6, v232
v_cmp_gt_u32_e32 vcc, 8, v22
s_add_u32 s67, s66, 0x80
s_cmp_eq_u32 s67, 0x500
s_cselect_b32 s67, 0, s67
s_lshl4_add_u32 m0, s67, s56
s_nop 0
buffer_load_dwordx4 v28, s[8:11], s67 offen lds
v_or_b32 v232, s84, v232
s_mul_i32 s43, s63, 0x500
s_add_u32 s43, s43, s50
v_add_u32 v72, s75, v0
s_waitcnt vmcnt(13)
ds_read_b128 v[40:43], v72
s_lshl4_add_u32 m0, s67, s76
s_nop 0
buffer_load_dwordx4 v26, s[8:11], s67 offen lds
v_cndmask_b32 v232, v9, v232, vcc
v_add_u32 v37, 0, v20
v_mul_u32_u24 v38, 0xcd, v37
s_waitcnt vmcnt(4)
s_waitcnt lgkmcnt(0)
s_barrier
buffer_store_dwordx2 v[4:5], v2, s[4:7], s55 offen sc1
ds_read_b128 v[44:47], v72 offset:2048
s_add_u32 s68, s67, 0x80
s_cmp_eq_u32 s68, 0x500
s_cselect_b32 s68, 0, s68
s_lshl4_add_u32 m0, s68, s56
s_nop 0
buffer_load_dwordx4 v28, s[8:11], s68 offen lds
v_lshrrev_b32 v38, 11, v38
v_mul_u32_u24 v38, 0x460, v38
v_lshl_add_u32 v37, v37, 4, s43
ds_read_b128 v[48:51], v72 offset:4096
v_dot2c_f32_bf16 v184, s91, v40
v_dot2c_f32_bf16 v185, v40, v40
v_dot2c_f32_bf16 v184, s91, v41
v_dot2c_f32_bf16 v185, v41, v41
v_dot2c_f32_bf16 v184, s91, v42
v_dot2c_f32_bf16 v185, v42, v42
v_dot2c_f32_bf16 v184, s91, v43
v_dot2c_f32_bf16 v185, v43, v43
s_lshl4_add_u32 m0, s68, s76
s_nop 0
buffer_load_dwordx4 v26, s[8:11], s68 offen lds
v_add_u32 v6, v37, v38
v_add_u32 v37, 64, v20
v_mul_u32_u24 v38, 0xcd, v37
ds_read_b128 v[52:55], v72 offset:6144
s_waitcnt lgkmcnt(2)
v_dot2c_f32_bf16 v184, s91, v44
v_dot2c_f32_bf16 v185, v44, v44
v_dot2c_f32_bf16 v184, s91, v45
v_dot2c_f32_bf16 v185, v45, v45
v_dot2c_f32_bf16 v184, s91, v46
v_dot2c_f32_bf16 v185, v46, v46
v_dot2c_f32_bf16 v184, s91, v47
v_dot2c_f32_bf16 v185, v47, v47
s_add_u32 s69, s68, 0x80
s_cmp_eq_u32 s69, 0x500
s_cselect_b32 s69, 0, s69
s_lshl4_add_u32 m0, s69, s56
s_nop 0
buffer_load_dwordx4 v28, s[8:11], s69 offen lds
v_lshrrev_b32 v38, 11, v38
v_mul_u32_u24 v38, 0x460, v38
v_lshl_add_u32 v37, v37, 4, s43
ds_read_b128 v[56:59], v72 offset:8192
ds_write_b128 v213, v[76:79]
s_waitcnt lgkmcnt(3)
v_dot2c_f32_bf16 v184, s91, v48
v_dot2c_f32_bf16 v185, v48, v48
v_dot2c_f32_bf16 v184, s91, v49
v_dot2c_f32_bf16 v185, v49, v49
v_dot2c_f32_bf16 v184, s91, v50
v_dot2c_f32_bf16 v185, v50, v50
v_dot2c_f32_bf16 v184, s91, v51
v_dot2c_f32_bf16 v185, v51, v51
s_lshl4_add_u32 m0, s69, s76
s_nop 0
buffer_load_dwordx4 v26, s[8:11], s69 offen lds
v_add_u32 v7, v37, v38
v_add_u32 v37, 0x80, v20
v_mul_u32_u24 v38, 0xcd, v37
s_lshl4_add_u32 s42, s65, s56
v_add_u32 v73, s42, v0
ds_read_b128 v[60:63], v73
s_waitcnt lgkmcnt(3)
v_dot2c_f32_bf16 v184, s91, v52
v_dot2c_f32_bf16 v185, v52, v52
v_dot2c_f32_bf16 v184, s91, v53
v_dot2c_f32_bf16 v185, v53, v53
v_dot2c_f32_bf16 v184, s91, v54
v_dot2c_f32_bf16 v185, v54, v54
v_dot2c_f32_bf16 v184, s91, v55
v_dot2c_f32_bf16 v185, v55, v55
s_add_u32 s70, s69, 0x80
s_cmp_eq_u32 s70, 0x500
s_cselect_b32 s70, 0, s70
s_lshl4_add_u32 m0, s70, s56
s_nop 0
buffer_load_dwordx4 v28, s[8:11], s70 offen lds
v_lshrrev_b32 v38, 11, v38
v_mul_u32_u24 v38, 0x460, v38
v_lshl_add_u32 v37, v37, 4, s43
ds_read_b128 v[64:67], v73 offset:1024
s_waitcnt lgkmcnt(3)
v_dot2c_f32_bf16 v184, s91, v56
v_dot2c_f32_bf16 v185, v56, v56
v_dot2c_f32_bf16 v184, s91, v57
v_dot2c_f32_bf16 v185, v57, v57
v_dot2c_f32_bf16 v184, s91, v58
v_dot2c_f32_bf16 v185, v58, v58
v_dot2c_f32_bf16 v184, s91, v59
v_dot2c_f32_bf16 v185, v59, v59
s_lshl4_add_u32 m0, s70, s76
s_nop 0
buffer_load_dwordx4 v26, s[8:11], s70 offen lds
v_add_u32 v8, v37, v38
v_cmp_gt_u32_e32 vcc, 32, v20
s_nop 0
s_lshl4_add_u32 s42, s66, s56
v_add_u32 v72, s42, v0
s_waitcnt vmcnt(10)
ds_read_b128 v[68:71], v72
s_waitcnt lgkmcnt(2)
v_dot2c_f32_bf16 v202, s91, v60
v_dot2c_f32_bf16 v203, v60, v60
v_dot2c_f32_bf16 v202, s91, v61
v_dot2c_f32_bf16 v203, v61, v61
v_dot2c_f32_bf16 v202, s91, v62
v_dot2c_f32_bf16 v203, v62, v62
v_dot2c_f32_bf16 v202, s91, v63
v_dot2c_f32_bf16 v203, v63, v63
v_add_f32_dpp v184, v184, v184 quad_perm:[1,0,3,2] row_mask:0xf bank_mask:0xf bound_ctrl:1
v_add_f32_dpp v185, v185, v185 quad_perm:[1,0,3,2] row_mask:0xf bank_mask:0xf bound_ctrl:1
s_nop 0
v_add_f32_dpp v184, v184, v184 quad_perm:[2,3,0,1] row_mask:0xf bank_mask:0xf bound_ctrl:1
v_add_f32_dpp v185, v185, v185 quad_perm:[2,3,0,1] row_mask:0xf bank_mask:0xf bound_ctrl:1
s_nop 0
v_add_f32_dpp v184, v184, v184 row_half_mirror row_mask:0xf bank_mask:0xf bound_ctrl:1
v_add_f32_dpp v185, v185, v185 row_half_mirror row_mask:0xf bank_mask:0xf bound_ctrl:1
ds_write_b64 v211, v[184:185]
s_add_u32 s71, s70, 0x80
s_cmp_eq_u32 s71, 0x500
s_cselect_b32 s71, 0, s71
s_lshl4_add_u32 m0, s71, s56
s_nop 0
buffer_load_dwordx4 v28, s[8:11], s71 offen lds
v_cndmask_b32 v8, v9, v8, vcc
s_lshl_b32 s43, s36, 9
s_lshl_b32 s44, s35, 7
s_waitcnt vmcnt(10)
ds_read_b128 v[40:43], v72 offset:1024
s_waitcnt lgkmcnt(3)
v_dot2c_f32_bf16 v204, s91, v64
v_dot2c_f32_bf16 v205, v64, v64
v_dot2c_f32_bf16 v204, s91, v65
v_dot2c_f32_bf16 v205, v65, v65
v_dot2c_f32_bf16 v204, s91, v66
v_dot2c_f32_bf16 v205, v66, v66
v_dot2c_f32_bf16 v204, s91, v67
v_dot2c_f32_bf16 v205, v67, v67
s_lshl4_add_u32 m0, s71, s76
s_nop 0
buffer_load_dwordx4 v26, s[8:11], s71 offen lds
s_lshl_b32 s45, s37, 7
v_lshrrev_b32 v37, 2, v20
v_and_b32 v38, 3, v20
s_lshl4_add_u32 s42, s67, s56
v_add_u32 v73, s42, v0
s_waitcnt vmcnt(10)
ds_read_b128 v[44:47], v73
s_waitcnt lgkmcnt(3)
v_dot2c_f32_bf16 v202, s91, v68
v_dot2c_f32_bf16 v203, v68, v68
v_dot2c_f32_bf16 v202, s91, v69
v_dot2c_f32_bf16 v203, v69, v69
v_dot2c_f32_bf16 v202, s91, v70
v_dot2c_f32_bf16 v203, v70, v70
v_dot2c_f32_bf16 v202, s91, v71
v_dot2c_f32_bf16 v203, v71, v71
s_add_u32 s72, s71, 0x80
s_cmp_eq_u32 s72, 0x500
s_cselect_b32 s72, 0, s72
s_lshl4_add_u32 m0, s72, s56
s_nop 0
)ASM"
R"ASM(
buffer_load_dwordx4 v28, s[8:11], s72 offen lds
v_lshlrev_b32 v37, 12, v37
v_lshl_add_u32 v38, v38, 7, s43
v_add_u32 v37, v37, v38
s_waitcnt vmcnt(10)
ds_read_b128 v[48:51], v73 offset:1024
s_waitcnt lgkmcnt(2)
v_dot2c_f32_bf16 v204, s91, v40
v_dot2c_f32_bf16 v205, v40, v40
v_dot2c_f32_bf16 v204, s91, v41
v_dot2c_f32_bf16 v205, v41, v41
v_dot2c_f32_bf16 v204, s91, v42
v_dot2c_f32_bf16 v205, v42, v42
v_dot2c_f32_bf16 v204, s91, v43
v_dot2c_f32_bf16 v205, v43, v43
s_lshl4_add_u32 m0, s72, s76
s_nop 0
buffer_load_dwordx4 v26, s[8:11], s72 offen lds
v_mov_b32 v38, s45
v_mov_b32 v32, s44
v_cmp_eq_u32_e32 vcc, 8, v20
s_lshl4_add_u32 s42, s68, s56
v_add_u32 v72, s42, v0
s_waitcnt vmcnt(9)
ds_read_b128 v[52:55], v72
s_waitcnt lgkmcnt(2)
v_dot2c_f32_bf16 v202, s91, v44
v_dot2c_f32_bf16 v203, v44, v44
v_dot2c_f32_bf16 v202, s91, v45
v_dot2c_f32_bf16 v203, v45, v45
v_dot2c_f32_bf16 v202, s91, v46
v_dot2c_f32_bf16 v203, v46, v46
v_dot2c_f32_bf16 v202, s91, v47
v_dot2c_f32_bf16 v203, v47, v47
s_add_u32 s73, s72, 0x80
s_cmp_eq_u32 s73, 0x500
s_cselect_b32 s73, 0, s73
s_lshl4_add_u32 m0, s73, s56
s_nop 0
buffer_load_dwordx4 v28, s[8:11], s73 offen lds
v_add_u32 v39, -6, v20
s_nop 0
v_cndmask_b32 v38, v38, v32, vcc
s_waitcnt vmcnt(9)
ds_read_b128 v[56:59], v72 offset:1024
s_waitcnt lgkmcnt(2)
v_dot2c_f32_bf16 v204, s91, v48
v_dot2c_f32_bf16 v205, v48, v48
v_dot2c_f32_bf16 v204, s91, v49
v_dot2c_f32_bf16 v205, v49, v49
v_dot2c_f32_bf16 v204, s91, v50
v_dot2c_f32_bf16 v205, v50, v50
v_dot2c_f32_bf16 v204, s91, v51
v_dot2c_f32_bf16 v205, v51, v51
s_lshl4_add_u32 m0, s73, s76
s_nop 0
buffer_load_dwordx4 v26, s[8:11], s73 offen lds
v_cmp_gt_u32_e32 vcc, 8, v20
v_lshl_add_u32 v38, v39, 12, v38
s_nop 0
s_lshl4_add_u32 s42, s69, s56
v_add_u32 v73, s42, v0
s_waitcnt vmcnt(9)
ds_read_b128 v[60:63], v73
s_waitcnt lgkmcnt(2)
v_dot2c_f32_bf16 v202, s91, v52
v_dot2c_f32_bf16 v203, v52, v52
v_dot2c_f32_bf16 v202, s91, v53
v_dot2c_f32_bf16 v203, v53, v53
v_dot2c_f32_bf16 v202, s91, v54
v_dot2c_f32_bf16 v203, v54, v54
v_dot2c_f32_bf16 v202, s91, v55
v_dot2c_f32_bf16 v203, v55, v55
s_add_u32 s74, s73, 0x80
s_cmp_eq_u32 s74, 0x500
s_cselect_b32 s74, 0, s74
s_lshl4_add_u32 m0, s74, s56
s_nop 0
buffer_load_dwordx4 v28, s[8:11], s74 offen lds
v_cndmask_b32 v37, v38, v37, vcc
v_cmp_gt_u32_e32 vcc, 13, v20
s_cmp_eq_u32 s34, 3
s_cselect_b32 s43, 0, 0x80000000
s_waitcnt vmcnt(9)
ds_read_b128 v[64:67], v73 offset:1024
s_waitcnt lgkmcnt(2)
v_dot2c_f32_bf16 v204, s91, v56
v_dot2c_f32_bf16 v205, v56, v56
v_dot2c_f32_bf16 v204, s91, v57
v_dot2c_f32_bf16 v205, v57, v57
v_dot2c_f32_bf16 v204, s91, v58
v_dot2c_f32_bf16 v205, v58, v58
v_dot2c_f32_bf16 v204, s91, v59
v_dot2c_f32_bf16 v205, v59, v59
s_lshl4_add_u32 m0, s74, s76
s_nop 0
buffer_load_dwordx4 v26, s[8:11], s74 offen lds
v_cndmask_b32 v37, v9, v37, vcc
v_or_b32 v37, s43, v37
s_mov_b32 m0, 0x24600
s_nop 0
buffer_load_dword v37, s[4:7], 0 offen lds
s_lshl4_add_u32 s42, s70, s56
v_add_u32 v72, s42, v0
s_waitcnt vmcnt(10)
ds_read_b128 v[68:71], v72
s_waitcnt lgkmcnt(2)
v_dot2c_f32_bf16 v202, s91, v60
v_dot2c_f32_bf16 v203, v60, v60
v_dot2c_f32_bf16 v202, s91, v61
v_dot2c_f32_bf16 v203, v61, v61
v_dot2c_f32_bf16 v202, s91, v62
v_dot2c_f32_bf16 v203, v62, v62
v_dot2c_f32_bf16 v202, s91, v63
v_dot2c_f32_bf16 v203, v63, v63
s_mov_b32 s12, s16
s_and_b32 s13, s17, 0xffff
s_mov_b32 s14, s18
s_mov_b32 s15, 0x20000
s_lshl_b32 s44, s79, 2
s_add_u32 m0, s90, 0
s_nop 0
buffer_load_dwordx4 v30, s[12:15], s80 offen lds
s_waitcnt vmcnt(10)
ds_read_b128 v[40:43], v72 offset:1024
s_waitcnt lgkmcnt(2)
v_dot2c_f32_bf16 v204, s91, v64
v_dot2c_f32_bf16 v205, v64, v64
v_dot2c_f32_bf16 v204, s91, v65
v_dot2c_f32_bf16 v205, v65, v65
v_dot2c_f32_bf16 v204, s91, v66
v_dot2c_f32_bf16 v205, v66, v66
v_dot2c_f32_bf16 v204, s91, v67
v_dot2c_f32_bf16 v205, v67, v67
s_add_u32 m0, s90, 0x400
s_add_u32 s80, s80, s44
buffer_load_dwordx4 v27, s[12:15], s80 offen lds
s_lshl4_add_u32 s42, s71, s56
v_add_u32 v73, s42, v0
s_waitcnt vmcnt(10)
ds_read_b128 v[44:47], v73
s_waitcnt lgkmcnt(2)
v_dot2c_f32_bf16 v202, s91, v68
v_dot2c_f32_bf16 v203, v68, v68
v_dot2c_f32_bf16 v202, s91, v69
v_dot2c_f32_bf16 v203, v69, v69
v_dot2c_f32_bf16 v202, s91, v70
v_dot2c_f32_bf16 v203, v70, v70
v_dot2c_f32_bf16 v202, s91, v71
v_dot2c_f32_bf16 v203, v71, v71
s_add_u32 m0, s90, 0x800
s_add_u32 s80, s80, s44
buffer_load_dwordx4 v30, s[12:15], s80 offen lds
s_waitcnt vmcnt(10)
ds_read_b128 v[48:51], v73 offset:1024
s_waitcnt lgkmcnt(2)
v_dot2c_f32_bf16 v204, s91, v40
v_dot2c_f32_bf16 v205, v40, v40
v_dot2c_f32_bf16 v204, s91, v41
v_dot2c_f32_bf16 v205, v41, v41
v_dot2c_f32_bf16 v204, s91, v42
v_dot2c_f32_bf16 v205, v42, v42
v_dot2c_f32_bf16 v204, s91, v43
v_dot2c_f32_bf16 v205, v43, v43
s_add_u32 m0, s90, 0xc00
s_add_u32 s80, s80, s44
buffer_load_dwordx4 v27, s[12:15], s80 offen lds
s_lshl4_add_u32 s42, s72, s56
v_add_u32 v72, s42, v0
s_waitcnt vmcnt(10)
ds_read_b128 v[52:55], v72
s_waitcnt lgkmcnt(2)
v_dot2c_f32_bf16 v202, s91, v44
v_dot2c_f32_bf16 v203, v44, v44
v_dot2c_f32_bf16 v202, s91, v45
v_dot2c_f32_bf16 v203, v45, v45
v_dot2c_f32_bf16 v202, s91, v46
v_dot2c_f32_bf16 v203, v46, v46
v_dot2c_f32_bf16 v202, s91, v47
v_dot2c_f32_bf16 v203, v47, v47
s_add_u32 m0, s90, 0x1000
s_nop 0
buffer_load_dwordx4 v30, s[12:15], s81 offen lds
s_waitcnt vmcnt(10)
ds_read_b128 v[56:59], v72 offset:1024
s_waitcnt lgkmcnt(2)
v_dot2c_f32_bf16 v204, s91, v48
v_dot2c_f32_bf16 v205, v48, v48
v_dot2c_f32_bf16 v204, s91, v49
v_dot2c_f32_bf16 v205, v49, v49
v_dot2c_f32_bf16 v204, s91, v50
v_dot2c_f32_bf16 v205, v50, v50
v_dot2c_f32_bf16 v204, s91, v51
v_dot2c_f32_bf16 v205, v51, v51
s_add_u32 m0, s90, 0x1400
s_add_u32 s81, s81, s44
buffer_load_dwordx4 v27, s[12:15], s81 offen lds
s_lshl4_add_u32 s42, s73, s56
v_add_u32 v73, s42, v0
s_waitcnt vmcnt(10)
)ASM"
R"ASM(
ds_read_b128 v[60:63], v73
s_waitcnt lgkmcnt(2)
v_dot2c_f32_bf16 v202, s91, v52
v_dot2c_f32_bf16 v203, v52, v52
v_dot2c_f32_bf16 v202, s91, v53
v_dot2c_f32_bf16 v203, v53, v53
v_dot2c_f32_bf16 v202, s91, v54
v_dot2c_f32_bf16 v203, v54, v54
v_dot2c_f32_bf16 v202, s91, v55
v_dot2c_f32_bf16 v203, v55, v55
s_add_u32 m0, s90, 0x1800
s_add_u32 s81, s81, s44
buffer_load_dwordx4 v30, s[12:15], s81 offen lds
s_waitcnt vmcnt(10)
ds_read_b128 v[64:67], v73 offset:1024
s_waitcnt lgkmcnt(2)
v_dot2c_f32_bf16 v204, s91, v56
v_dot2c_f32_bf16 v205, v56, v56
v_dot2c_f32_bf16 v204, s91, v57
v_dot2c_f32_bf16 v205, v57, v57
v_dot2c_f32_bf16 v204, s91, v58
v_dot2c_f32_bf16 v205, v58, v58
v_dot2c_f32_bf16 v204, s91, v59
v_dot2c_f32_bf16 v205, v59, v59
s_add_u32 m0, s90, 0x1c00
s_add_u32 s81, s81, s44
buffer_load_dwordx4 v27, s[12:15], s81 offen lds
s_lshl4_add_u32 s42, s74, s56
v_add_u32 v72, s42, v0
s_waitcnt vmcnt(10)
ds_read_b128 v[68:71], v72
s_waitcnt lgkmcnt(2)
v_dot2c_f32_bf16 v202, s91, v60
v_dot2c_f32_bf16 v203, v60, v60
v_dot2c_f32_bf16 v202, s91, v61
v_dot2c_f32_bf16 v203, v61, v61
v_dot2c_f32_bf16 v202, s91, v62
v_dot2c_f32_bf16 v203, v62, v62
v_dot2c_f32_bf16 v202, s91, v63
v_dot2c_f32_bf16 v203, v63, v63
s_waitcnt vmcnt(9)
ds_read_b128 v[40:43], v72 offset:1024
buffer_load_dwordx2 v[226:227], v3, s[4:7], 0 offen sc1
s_waitcnt lgkmcnt(2)
v_dot2c_f32_bf16 v204, s91, v64
v_dot2c_f32_bf16 v205, v64, v64
v_dot2c_f32_bf16 v204, s91, v65
v_dot2c_f32_bf16 v205, v65, v65
v_dot2c_f32_bf16 v204, s91, v66
v_dot2c_f32_bf16 v205, v66, v66
v_dot2c_f32_bf16 v204, s91, v67
v_dot2c_f32_bf16 v205, v67, v67
s_waitcnt lgkmcnt(1)
v_dot2c_f32_bf16 v202, s91, v68
v_dot2c_f32_bf16 v203, v68, v68
v_dot2c_f32_bf16 v202, s91, v69
v_dot2c_f32_bf16 v203, v69, v69
v_dot2c_f32_bf16 v202, s91, v70
v_dot2c_f32_bf16 v203, v70, v70
v_dot2c_f32_bf16 v202, s91, v71
v_dot2c_f32_bf16 v203, v71, v71
s_waitcnt lgkmcnt(0)
v_dot2c_f32_bf16 v204, s91, v40
v_dot2c_f32_bf16 v205, v40, v40
v_dot2c_f32_bf16 v204, s91, v41
v_dot2c_f32_bf16 v205, v41, v41
v_dot2c_f32_bf16 v204, s91, v42
v_dot2c_f32_bf16 v205, v42, v42
v_dot2c_f32_bf16 v204, s91, v43
v_dot2c_f32_bf16 v205, v43, v43
s_nop 1
v_add_f32_dpp v202, v202, v202 quad_perm:[1,0,3,2] row_mask:0xf bank_mask:0xf bound_ctrl:1
v_add_f32_dpp v203, v203, v203 quad_perm:[1,0,3,2] row_mask:0xf bank_mask:0xf bound_ctrl:1
v_add_f32_dpp v204, v204, v204 quad_perm:[1,0,3,2] row_mask:0xf bank_mask:0xf bound_ctrl:1
v_add_f32_dpp v205, v205, v205 quad_perm:[1,0,3,2] row_mask:0xf bank_mask:0xf bound_ctrl:1
v_add_f32_dpp v202, v202, v202 quad_perm:[2,3,0,1] row_mask:0xf bank_mask:0xf bound_ctrl:1
v_add_f32_dpp v203, v203, v203 quad_perm:[2,3,0,1] row_mask:0xf bank_mask:0xf bound_ctrl:1
v_add_f32_dpp v204, v204, v204 quad_perm:[2,3,0,1] row_mask:0xf bank_mask:0xf bound_ctrl:1
v_add_f32_dpp v205, v205, v205 quad_perm:[2,3,0,1] row_mask:0xf bank_mask:0xf bound_ctrl:1
v_add_f32_dpp v202, v202, v202 row_half_mirror row_mask:0xf bank_mask:0xf bound_ctrl:1
v_add_f32_dpp v203, v203, v203 row_half_mirror row_mask:0xf bank_mask:0xf bound_ctrl:1
v_add_f32_dpp v204, v204, v204 row_half_mirror row_mask:0xf bank_mask:0xf bound_ctrl:1
v_add_f32_dpp v205, v205, v205 row_half_mirror row_mask:0xf bank_mask:0xf bound_ctrl:1
v_mul_f32_e64 v202, -v202, s57
v_mul_f32 v203, s57, v203
v_fma_f32 v203, -v202, v202, v203
v_max_f32 v203, 0, v203
v_add_f32 v203, s58, v203
v_rsq_f32 v203, v203
v_mul_f32_e64 v204, -v204, s57
v_mul_f32 v205, s57, v205
v_fma_f32 v205, -v204, v204, v205
v_max_f32 v205, 0, v205
v_add_f32 v205, s58, v205
v_rsq_f32 v205, v205
ds_write_b32 v212, v202
ds_write_b32 v212, v203 offset:320
ds_write_b32 v212, v204 offset:32
ds_write_b32 v212, v205 offset:352
v_mov_b32 v234, v210
v_lshrrev_b32 v236, 8, v0
v_and_b32 v238, 6, v233
v_xor_b32 v238, v238, v236
v_lshlrev_b32 v238, 4, v238
v_lshl_or_b32 v20, v233, 7, v238
v_xor_b32 v21, 64, v20
v_add_u32 v22, 0xf000, v20
v_add_u32 v23, 0xf000, v21
s_waitcnt vmcnt(0)
v_cmp_eq_u64_e64 s[94:95], s[40:41], v[226:227]
s_nop 0
s_cmp_eq_u64 s[94:95], exec
s_cbranch_scc0 .Lk_fws_1
.Lk_fwb_2:
buffer_load_dwordx4 a[92:95], v0, s[4:7], s39 offen sc1
buffer_load_dwordx4 a[96:99], v0, s[4:7], s39 offen offset:1024 sc1
buffer_load_dwordx4 a[100:103], v0, s[4:7], s39 offen offset:2048 sc1
buffer_load_dwordx4 a[104:107], v161, s[4:7], s39 offen offset:3072 sc1
s_add_u32 s39, s39, 0xf00
buffer_load_dwordx4 a[108:111], v0, s[4:7], s39 offen sc1
buffer_load_dwordx4 a[112:115], v0, s[4:7], s39 offen offset:1024 sc1
buffer_load_dwordx4 a[116:119], v0, s[4:7], s39 offen offset:2048 sc1
buffer_load_dwordx4 a[120:123], v161, s[4:7], s39 offen offset:3072 sc1
s_add_u32 s39, s39, 0xf00
buffer_load_dwordx4 a[124:127], v0, s[4:7], s39 offen sc1
buffer_load_dwordx4 a[128:131], v0, s[4:7], s39 offen offset:1024 sc1
buffer_load_dwordx4 a[132:135], v0, s[4:7], s39 offen offset:2048 sc1
buffer_load_dwordx4 a[136:139], v161, s[4:7], s39 offen offset:3072 sc1
s_add_u32 s39, s39, 0xf00
s_waitcnt lgkmcnt(0)
s_barrier
ds_read_b128 v[128:131], v20
ds_read_b128 v[132:135], v20 offset:20480
ds_read_b128 v[136:139], v20 offset:40960
ds_read_b128 v[140:143], v22
ds_read_b128 v[144:147], v22 offset:20480
s_waitcnt vmcnt(8) lgkmcnt(4)
v_mfma_f32_16x16x32_bf16 v[48:51], a[92:95], v[128:131], 0
v_mfma_f32_16x16x32_bf16 v[68:71], a[96:99], v[128:131], 0
ds_read_b128 v[148:151], v21
v_mfma_f32_16x16x32_bf16 v[88:91], v[128:131], a[100:103], 0
buffer_load_dwordx4 a[140:143], v0, s[4:7], s39 offen sc1
v_mfma_f32_16x16x32_bf16 v[108:111], a[104:107], v[128:131], 0
s_waitcnt lgkmcnt(4)
v_mfma_f32_16x16x32_bf16 v[52:55], a[92:95], v[132:135], 0
v_mfma_f32_16x16x32_bf16 v[72:75], a[96:99], v[132:135], 0
ds_read_b128 v[128:131], v21 offset:20480
v_mfma_f32_16x16x32_bf16 v[92:95], v[132:135], a[100:103], 0
buffer_load_dwordx4 a[144:147], v0, s[4:7], s39 offen offset:1024 sc1
v_mfma_f32_16x16x32_bf16 v[112:115], a[104:107], v[132:135], 0
s_waitcnt lgkmcnt(4)
v_mfma_f32_16x16x32_bf16 v[56:59], a[92:95], v[136:139], 0
v_mfma_f32_16x16x32_bf16 v[76:79], a[96:99], v[136:139], 0
ds_read_b128 v[132:135], v21 offset:40960
v_mfma_f32_16x16x32_bf16 v[96:99], v[136:139], a[100:103], 0
buffer_load_dwordx4 a[148:151], v0, s[4:7], s39 offen offset:2048 sc1
v_mfma_f32_16x16x32_bf16 v[116:119], a[104:107], v[136:139], 0
s_waitcnt lgkmcnt(4)
v_mfma_f32_16x16x32_bf16 v[60:63], a[92:95], v[140:143], 0
v_mfma_f32_16x16x32_bf16 v[80:83], a[96:99], v[140:143], 0
ds_read_b128 v[136:139], v23
v_mfma_f32_16x16x32_bf16 v[100:103], v[140:143], a[100:103], 0
buffer_load_dwordx4 a[152:155], v161, s[4:7], s39 offen offset:3072 sc1
s_add_u32 s39, s39, 0xf00
v_mfma_f32_16x16x32_bf16 v[120:123], a[104:107], v[140:143], 0
s_waitcnt lgkmcnt(4)
v_mfma_f32_16x16x32_bf16 v[64:67], a[92:95], v[144:147], 0
v_mfma_f32_16x16x32_bf16 v[84:87], a[96:99], v[144:147], 0
ds_read_b128 v[140:143], v23 offset:20480
v_mfma_f32_16x16x32_bf16 v[104:107], v[144:147], a[100:103], 0
v_mfma_f32_16x16x32_bf16 v[124:127], a[104:107], v[144:147], 0
s_waitcnt vmcnt(8) lgkmcnt(4)
v_mfma_f32_16x16x32_bf16 v[48:51], a[108:111], v[148:151], v[48:51]
v_mov_b32 v208, s91
ds_read_b64_tr_b16 v[168:169], v228
v_mfma_f32_16x16x32_bf16 v[68:71], a[112:115], v[148:151], v[68:71]
ds_read_b128 v[144:147], v20 offset:2048
ds_read_b64_tr_b16 v[170:171], v229
v_mfma_f32_16x16x32_bf16 v[88:91], v[148:151], a[116:119], v[88:91]
buffer_load_dwordx4 a[92:95], v0, s[4:7], s39 offen sc1
v_mfma_f32_16x16x32_bf16 v[108:111], a[120:123], v[148:151], v[108:111]
v_xor_b32 v211, 32, v228
s_waitcnt lgkmcnt(6)
v_mfma_f32_16x16x32_bf16 v[52:55], a[108:111], v[128:131], v[52:55]
v_xor_b32 v212, 32, v229
ds_read_b64_tr_b16 v[172:173], v211
v_mfma_f32_16x16x32_bf16 v[72:75], a[112:115], v[128:131], v[72:75]
ds_read_b128 v[148:151], v20 offset:22528
ds_read_b64_tr_b16 v[174:175], v212
v_mfma_f32_16x16x32_bf16 v[92:95], v[128:131], a[116:119], v[92:95]
buffer_load_dwordx4 a[96:99], v0, s[4:7], s39 offen offset:1024 sc1
v_mfma_f32_16x16x32_bf16 v[112:115], a[120:123], v[128:131], v[112:115]
v_xor_b32 v209, 64, v228
s_waitcnt lgkmcnt(8)
v_mfma_f32_16x16x32_bf16 v[56:59], a[108:111], v[132:135], v[56:59]
v_xor_b32 v210, 64, v229
ds_read_b64_tr_b16 v[176:177], v209
v_mfma_f32_16x16x32_bf16 v[76:79], a[112:115], v[132:135], v[76:79]
ds_read_b128 v[128:131], v20 offset:43008
ds_read_b64_tr_b16 v[178:179], v210
)ASM"
R"ASM(
v_mfma_f32_16x16x32_bf16 v[96:99], v[132:135], a[116:119], v[96:99]
buffer_load_dwordx4 a[100:103], v0, s[4:7], s39 offen offset:2048 sc1
v_mfma_f32_16x16x32_bf16 v[116:119], a[120:123], v[132:135], v[116:119]
v_xor_b32 v211, 0x60, v228
s_waitcnt lgkmcnt(10)
v_mfma_f32_16x16x32_bf16 v[60:63], a[108:111], v[136:139], v[60:63]
v_xor_b32 v212, 0x60, v229
ds_read_b64_tr_b16 v[180:181], v211
v_mfma_f32_16x16x32_bf16 v[80:83], a[112:115], v[136:139], v[80:83]
ds_read_b128 v[132:135], v22 offset:2048
ds_read_b64_tr_b16 v[182:183], v212
v_mfma_f32_16x16x32_bf16 v[100:103], v[136:139], a[116:119], v[100:103]
buffer_load_dwordx4 a[104:107], v161, s[4:7], s39 offen offset:3072 sc1
s_add_u32 s39, s39, 0xf00
v_mfma_f32_16x16x32_bf16 v[120:123], a[120:123], v[136:139], v[120:123]
v_xor_b32 v209, 0x80, v228
s_waitcnt lgkmcnt(12)
v_mfma_f32_16x16x32_bf16 v[64:67], a[108:111], v[140:143], v[64:67]
v_xor_b32 v210, 0x80, v229
ds_read_b64_tr_b16 v[184:185], v209
v_mfma_f32_16x16x32_bf16 v[84:87], a[112:115], v[140:143], v[84:87]
ds_read_b128 v[136:139], v22 offset:22528
ds_read_b64_tr_b16 v[186:187], v210
v_mfma_f32_16x16x32_bf16 v[104:107], v[140:143], a[116:119], v[104:107]
v_mfma_f32_16x16x32_bf16 v[124:127], a[120:123], v[140:143], v[124:127]
v_xor_b32 v211, 0xa0, v228
s_waitcnt vmcnt(8) lgkmcnt(13)
v_mfma_f32_16x16x32_bf16 v[48:51], a[124:127], v[144:147], v[48:51]
v_xor_b32 v212, 0xa0, v229
ds_read_b64_tr_b16 v[188:189], v211
v_mfma_f32_16x16x32_bf16 v[68:71], a[128:131], v[144:147], v[68:71]
ds_read_b128 v[140:143], v21 offset:2048
ds_read_b64_tr_b16 v[190:191], v212
v_mfma_f32_16x16x32_bf16 v[88:91], v[144:147], a[132:135], v[88:91]
buffer_load_dwordx4 a[108:111], v0, s[4:7], s39 offen sc1
v_mfma_f32_16x16x32_bf16 v[108:111], a[136:139], v[144:147], v[108:111]
v_xor_b32 v209, 0xc0, v228
s_waitcnt lgkmcnt(13)
v_mfma_f32_16x16x32_bf16 v[52:55], a[124:127], v[148:151], v[52:55]
v_xor_b32 v210, 0xc0, v229
ds_read_b64_tr_b16 v[192:193], v209
v_mfma_f32_16x16x32_bf16 v[72:75], a[128:131], v[148:151], v[72:75]
ds_read_b128 v[144:147], v21 offset:22528
ds_read_b64_tr_b16 v[194:195], v210
v_mfma_f32_16x16x32_bf16 v[92:95], v[148:151], a[132:135], v[92:95]
buffer_load_dwordx4 a[112:115], v0, s[4:7], s39 offen offset:1024 sc1
v_mfma_f32_16x16x32_bf16 v[112:115], a[136:139], v[148:151], v[112:115]
v_cmp_eq_u32_e64 s[92:93], 0, v233
s_waitcnt lgkmcnt(13)
v_mfma_f32_16x16x32_bf16 v[56:59], a[124:127], v[128:131], v[56:59]
s_add_u32 s55, s55, 0x1000
v_cndmask_b32_e64 v196, 0, v208, s[92:93]
v_mfma_f32_16x16x32_bf16 v[76:79], a[128:131], v[128:131], v[76:79]
ds_read_b128 v[148:151], v21 offset:43008
v_cndmask_b32_e64 v197, 0, v208, s[92:93]
v_mfma_f32_16x16x32_bf16 v[96:99], v[128:131], a[132:135], v[96:99]
buffer_load_dwordx4 a[116:119], v0, s[4:7], s39 offen offset:2048 sc1
v_mfma_f32_16x16x32_bf16 v[116:119], a[136:139], v[128:131], v[116:119]
v_cndmask_b32_e64 v198, 0, v208, s[92:93]
s_waitcnt lgkmcnt(11)
v_mfma_f32_16x16x32_bf16 v[60:63], a[124:127], v[132:135], v[60:63]
v_cndmask_b32_e64 v199, 0, v208, s[92:93]
v_cmp_eq_u32_e64 s[92:93], 1, v233
v_mfma_f32_16x16x32_bf16 v[80:83], a[128:131], v[132:135], v[80:83]
ds_read_b128 v[128:131], v23 offset:2048
v_mfma_f32_16x16x32_bf16 v[204:207], v[168:171], v[196:199], 0
v_mfma_f32_16x16x32_bf16 v[100:103], v[132:135], a[132:135], v[100:103]
buffer_load_dwordx4 a[120:123], v161, s[4:7], s39 offen offset:3072 sc1
s_add_u32 s39, s39, 0xf00
v_mfma_f32_16x16x32_bf16 v[120:123], a[136:139], v[132:135], v[120:123]
v_cndmask_b32_e64 v200, 0, v208, s[92:93]
s_waitcnt lgkmcnt(9)
v_mfma_f32_16x16x32_bf16 v[64:67], a[124:127], v[136:139], v[64:67]
v_cndmask_b32_e64 v201, 0, v208, s[92:93]
v_cndmask_b32_e64 v202, 0, v208, s[92:93]
v_mfma_f32_16x16x32_bf16 v[84:87], a[128:131], v[136:139], v[84:87]
ds_read_b128 v[132:135], v23 offset:22528
v_cndmask_b32_e64 v203, 0, v208, s[92:93]
v_mfma_f32_16x16x32_bf16 v[104:107], v[136:139], a[132:135], v[104:107]
buffer_store_dwordx4 v[168:171], v230, s[4:7], s77 offen sc1
s_add_u32 s77, s77, s82
v_mfma_f32_16x16x32_bf16 v[124:127], a[136:139], v[136:139], v[124:127]
v_cmp_eq_u32_e64 s[92:93], 2, v233
s_waitcnt vmcnt(9) lgkmcnt(7)
v_mfma_f32_16x16x32_bf16 v[48:51], a[140:143], v[140:143], v[48:51]
v_mfma_f32_16x16x32_bf16 v[204:207], v[172:175], v[200:203], v[204:207]
v_cndmask_b32_e64 v196, 0, v208, s[92:93]
v_mfma_f32_16x16x32_bf16 v[68:71], a[144:147], v[140:143], v[68:71]
ds_read_b128 v[136:139], v20 offset:4096
v_cndmask_b32_e64 v197, 0, v208, s[92:93]
v_mfma_f32_16x16x32_bf16 v[88:91], v[140:143], a[148:151], v[88:91]
buffer_load_dwordx4 a[124:127], v0, s[4:7], s39 offen sc1
v_mfma_f32_16x16x32_bf16 v[108:111], a[152:155], v[140:143], v[108:111]
v_cndmask_b32_e64 v198, 0, v208, s[92:93]
s_waitcnt lgkmcnt(5)
v_mfma_f32_16x16x32_bf16 v[52:55], a[140:143], v[144:147], v[52:55]
v_cndmask_b32_e64 v199, 0, v208, s[92:93]
v_cmp_eq_u32_e64 s[92:93], 3, v233
v_mfma_f32_16x16x32_bf16 v[72:75], a[144:147], v[144:147], v[72:75]
ds_read_b128 v[140:143], v20 offset:24576
v_mfma_f32_16x16x32_bf16 v[204:207], v[176:179], v[196:199], v[204:207]
v_mfma_f32_16x16x32_bf16 v[92:95], v[144:147], a[148:151], v[92:95]
buffer_load_dwordx4 a[128:131], v0, s[4:7], s39 offen offset:1024 sc1
buffer_store_dwordx4 v[172:175], v230, s[4:7], s77 offen sc1
s_add_u32 s77, s77, s82
v_mfma_f32_16x16x32_bf16 v[112:115], a[152:155], v[144:147], v[112:115]
v_cndmask_b32_e64 v200, 0, v208, s[92:93]
s_waitcnt lgkmcnt(4)
v_mfma_f32_16x16x32_bf16 v[56:59], a[140:143], v[148:151], v[56:59]
v_cndmask_b32_e64 v201, 0, v208, s[92:93]
v_cndmask_b32_e64 v202, 0, v208, s[92:93]
v_mfma_f32_16x16x32_bf16 v[76:79], a[144:147], v[148:151], v[76:79]
ds_read_b128 v[144:147], v20 offset:45056
v_cndmask_b32_e64 v203, 0, v208, s[92:93]
v_mfma_f32_16x16x32_bf16 v[96:99], v[148:151], a[148:151], v[96:99]
buffer_load_dwordx4 a[132:135], v0, s[4:7], s39 offen offset:2048 sc1
v_mfma_f32_16x16x32_bf16 v[116:119], a[152:155], v[148:151], v[116:119]
v_cmp_eq_u32_e64 s[92:93], 4, v233
s_waitcnt lgkmcnt(4)
v_mfma_f32_16x16x32_bf16 v[60:63], a[140:143], v[128:131], v[60:63]
v_mfma_f32_16x16x32_bf16 v[204:207], v[180:183], v[200:203], v[204:207]
v_cndmask_b32_e64 v196, 0, v208, s[92:93]
v_mfma_f32_16x16x32_bf16 v[80:83], a[144:147], v[128:131], v[80:83]
ds_read_b128 v[148:151], v22 offset:4096
v_cndmask_b32_e64 v197, 0, v208, s[92:93]
v_mfma_f32_16x16x32_bf16 v[100:103], v[128:131], a[148:151], v[100:103]
buffer_load_dwordx4 a[136:139], v161, s[4:7], s39 offen offset:3072 sc1
s_add_u32 s39, s39, 0xf00
v_mfma_f32_16x16x32_bf16 v[120:123], a[152:155], v[128:131], v[120:123]
v_cndmask_b32_e64 v198, 0, v208, s[92:93]
s_waitcnt lgkmcnt(4)
v_mfma_f32_16x16x32_bf16 v[64:67], a[140:143], v[132:135], v[64:67]
v_cndmask_b32_e64 v199, 0, v208, s[92:93]
v_cmp_eq_u32_e64 s[92:93], 5, v233
v_mfma_f32_16x16x32_bf16 v[84:87], a[144:147], v[132:135], v[84:87]
ds_read_b128 v[128:131], v22 offset:24576
v_mfma_f32_16x16x32_bf16 v[204:207], v[184:187], v[196:199], v[204:207]
v_mfma_f32_16x16x32_bf16 v[104:107], v[132:135], a[148:151], v[104:107]
buffer_store_dwordx4 v[176:179], v230, s[4:7], s77 offen sc1
s_add_u32 s77, s77, s82
v_mfma_f32_16x16x32_bf16 v[124:127], a[152:155], v[132:135], v[124:127]
v_cndmask_b32_e64 v200, 0, v208, s[92:93]
s_waitcnt vmcnt(11) lgkmcnt(4)
v_mfma_f32_16x16x32_bf16 v[48:51], a[92:95], v[136:139], v[48:51]
v_cndmask_b32_e64 v201, 0, v208, s[92:93]
v_cndmask_b32_e64 v202, 0, v208, s[92:93]
v_mfma_f32_16x16x32_bf16 v[68:71], a[96:99], v[136:139], v[68:71]
ds_read_b128 v[132:135], v21 offset:4096
v_cndmask_b32_e64 v203, 0, v208, s[92:93]
v_mfma_f32_16x16x32_bf16 v[88:91], v[136:139], a[100:103], v[88:91]
buffer_load_dwordx4 a[140:143], v0, s[4:7], s39 offen sc1
v_mfma_f32_16x16x32_bf16 v[108:111], a[104:107], v[136:139], v[108:111]
v_cmp_eq_u32_e64 s[92:93], 6, v233
s_waitcnt lgkmcnt(4)
v_mfma_f32_16x16x32_bf16 v[52:55], a[92:95], v[140:143], v[52:55]
v_mfma_f32_16x16x32_bf16 v[204:207], v[188:191], v[200:203], v[204:207]
v_cndmask_b32_e64 v196, 0, v208, s[92:93]
v_mfma_f32_16x16x32_bf16 v[72:75], a[96:99], v[140:143], v[72:75]
ds_read_b128 v[136:139], v21 offset:24576
v_cndmask_b32_e64 v197, 0, v208, s[92:93]
v_mfma_f32_16x16x32_bf16 v[92:95], v[140:143], a[100:103], v[92:95]
buffer_load_dwordx4 a[144:147], v0, s[4:7], s39 offen offset:1024 sc1
buffer_store_dwordx4 v[180:183], v230, s[4:7], s77 offen sc1
s_add_u32 s77, s77, s82
v_mfma_f32_16x16x32_bf16 v[112:115], a[104:107], v[140:143], v[112:115]
v_cndmask_b32_e64 v198, 0, v208, s[92:93]
s_waitcnt lgkmcnt(4)
v_mfma_f32_16x16x32_bf16 v[56:59], a[92:95], v[144:147], v[56:59]
v_cndmask_b32_e64 v199, 0, v208, s[92:93]
s_nop 1
v_mfma_f32_16x16x32_bf16 v[76:79], a[96:99], v[144:147], v[76:79]
ds_read_b128 v[140:143], v21 offset:45056
v_mfma_f32_16x16x32_bf16 v[204:207], v[192:195], v[196:199], v[204:207]
v_mfma_f32_16x16x32_bf16 v[96:99], v[144:147], a[100:103], v[96:99]
buffer_load_dwordx4 a[148:151], v0, s[4:7], s39 offen offset:2048 sc1
v_mfma_f32_16x16x32_bf16 v[116:119], a[104:107], v[144:147], v[116:119]
ds_read_b64 v[214:215], v234
s_waitcnt lgkmcnt(5)
v_mfma_f32_16x16x32_bf16 v[60:63], a[92:95], v[148:151], v[60:63]
ds_read_b64 v[216:217], v234 offset:128
v_lshlrev_b32 v235, 2, v233
v_mfma_f32_16x16x32_bf16 v[80:83], a[96:99], v[148:151], v[80:83]
ds_read_b128 v[144:147], v23 offset:4096
v_add_u32 v235, 0x24200, v235
v_mfma_f32_16x16x32_bf16 v[100:103], v[148:151], a[100:103], v[100:103]
buffer_load_dwordx4 a[152:155], v161, s[4:7], s39 offen offset:3072 sc1
s_add_u32 s39, s39, 0xf00
v_mfma_f32_16x16x32_bf16 v[120:123], a[104:107], v[148:151], v[120:123]
v_cmp_eq_u32_e32 vcc, 0, v233
s_waitcnt lgkmcnt(6)
v_mfma_f32_16x16x32_bf16 v[64:67], a[92:95], v[128:131], v[64:67]
v_lshlrev_b32 v46, 4, v236
v_add_u32 v46, 0x24200, v46
v_mfma_f32_16x16x32_bf16 v[84:87], a[96:99], v[128:131], v[84:87]
ds_read_b128 v[148:151], v23 offset:24576
s_waitcnt lgkmcnt(2)
v_add_f32 v152, v214, v216
v_mfma_f32_16x16x32_bf16 v[104:107], v[128:131], a[100:103], v[104:107]
buffer_store_dwordx4 v[184:187], v230, s[4:7], s77 offen sc1
s_add_u32 s77, s77, s82
)ASM"
R"ASM(
v_mfma_f32_16x16x32_bf16 v[124:127], a[104:107], v[128:131], v[124:127]
v_add_f32 v153, v215, v217
s_waitcnt vmcnt(13)
v_mfma_f32_16x16x32_bf16 v[48:51], a[108:111], v[132:135], v[48:51]
v_mul_f32_e64 v152, -v152, s57
v_mul_f32 v153, s57, v153
v_mfma_f32_16x16x32_bf16 v[68:71], a[112:115], v[132:135], v[68:71]
ds_read_b128 v[128:131], v20 offset:6144
v_fma_f32 v153, -v152, v152, v153
v_mfma_f32_16x16x32_bf16 v[88:91], v[132:135], a[116:119], v[88:91]
buffer_load_dwordx4 a[92:95], v0, s[4:7], s39 offen sc1
v_mfma_f32_16x16x32_bf16 v[108:111], a[120:123], v[132:135], v[108:111]
v_max_f32 v153, 0, v153
v_mfma_f32_16x16x32_bf16 v[52:55], a[108:111], v[136:139], v[52:55]
v_add_f32 v153, s58, v153
v_rsq_f32 v153, v153
v_mfma_f32_16x16x32_bf16 v[72:75], a[112:115], v[136:139], v[72:75]
ds_read_b128 v[132:135], v20 offset:26624
v_cndmask_b32_e64 v152, v152, 0, vcc
v_mfma_f32_16x16x32_bf16 v[92:95], v[136:139], a[116:119], v[92:95]
buffer_load_dwordx4 a[96:99], v0, s[4:7], s39 offen offset:1024 sc1
buffer_store_dwordx4 v[188:191], v231, s[4:7], s77 offen sc1
s_add_u32 s77, s77, s82
v_mfma_f32_16x16x32_bf16 v[112:115], a[120:123], v[136:139], v[112:115]
v_cndmask_b32_e64 v153, v153, 1.0, vcc
v_mfma_f32_16x16x32_bf16 v[56:59], a[108:111], v[140:143], v[56:59]
ds_write_b32 v235, v152
ds_write_b32 v235, v153 offset:320
v_mfma_f32_16x16x32_bf16 v[76:79], a[112:115], v[140:143], v[76:79]
ds_read_b128 v[136:139], v20 offset:47104
ds_read2_b32 v[154:155], v235 offset0:16 offset1:96
v_mfma_f32_16x16x32_bf16 v[96:99], v[140:143], a[116:119], v[96:99]
buffer_load_dwordx4 a[100:103], v0, s[4:7], s39 offen offset:2048 sc1
v_mfma_f32_16x16x32_bf16 v[116:119], a[120:123], v[140:143], v[116:119]
ds_read2_b32 v[162:163], v235 offset0:32 offset1:112
s_waitcnt lgkmcnt(8)
v_mfma_f32_16x16x32_bf16 v[60:63], a[108:111], v[144:147], v[60:63]
ds_read2_b32 v[164:165], v235 offset0:48 offset1:128
ds_read2_b32 v[166:167], v235 offset0:64 offset1:144
v_mfma_f32_16x16x32_bf16 v[80:83], a[112:115], v[144:147], v[80:83]
ds_read_b128 v[140:143], v22 offset:6144
s_mul_i32 s43, s36, 10
v_mfma_f32_16x16x32_bf16 v[100:103], v[144:147], a[116:119], v[100:103]
buffer_load_dwordx4 a[104:107], v161, s[4:7], s39 offen offset:3072 sc1
s_add_u32 s39, s39, 0xf00
v_mfma_f32_16x16x32_bf16 v[120:123], a[120:123], v[144:147], v[120:123]
v_lshrrev_b32 v238, 1, v236
s_waitcnt lgkmcnt(10)
v_mfma_f32_16x16x32_bf16 v[64:67], a[108:111], v[148:151], v[64:67]
v_add_u32 v238, s43, v238
v_and_b32 v239, 6, v233
v_mfma_f32_16x16x32_bf16 v[84:87], a[112:115], v[148:151], v[84:87]
ds_read_b128 v[144:147], v22 offset:26624
v_and_b32 v240, 1, v236
v_mfma_f32_16x16x32_bf16 v[104:107], v[148:151], a[116:119], v[104:107]
buffer_store_dwordx4 v[192:195], v231, s[4:7], s77 offen sc1
v_mfma_f32_16x16x32_bf16 v[124:127], a[120:123], v[148:151], v[124:127]
v_lshlrev_b32 v240, 3, v240
s_waitcnt vmcnt(13) lgkmcnt(10)
v_mfma_f32_16x16x32_bf16 v[48:51], a[124:127], v[128:131], v[48:51]
v_lshl_add_u32 v240, v233, 7, v240
v_add_u32 v240, s56, v240
v_mfma_f32_16x16x32_bf16 v[68:71], a[128:131], v[128:131], v[68:71]
ds_read_b128 v[148:151], v21 offset:6144
v_xor_b32 v241, v238, v239
v_mfma_f32_16x16x32_bf16 v[88:91], v[128:131], a[132:135], v[88:91]
buffer_load_dwordx4 a[108:111], v0, s[4:7], s39 offen sc1
v_mfma_f32_16x16x32_bf16 v[108:111], a[136:139], v[128:131], v[108:111]
v_and_b32 v241, 7, v241
s_waitcnt lgkmcnt(10)
v_mfma_f32_16x16x32_bf16 v[52:55], a[124:127], v[132:135], v[52:55]
v_lshl_add_u32 v241, v241, 4, v240
v_lshrrev_b32 v44, 3, v238
v_mfma_f32_16x16x32_bf16 v[72:75], a[128:131], v[132:135], v[72:75]
ds_read_b128 v[128:131], v21 offset:26624
v_lshl_add_u32 v241, v44, 11, v241
v_mfma_f32_16x16x32_bf16 v[92:95], v[132:135], a[132:135], v[92:95]
buffer_load_dwordx4 a[112:115], v0, s[4:7], s39 offen offset:1024 sc1
buffer_store_dwordx4 v[204:207], v232, s[4:7], s78 offen sc1
v_mfma_f32_16x16x32_bf16 v[112:115], a[136:139], v[132:135], v[112:115]
ds_read_b64 v[10:11], v241
s_waitcnt lgkmcnt(9)
v_mfma_f32_16x16x32_bf16 v[56:59], a[124:127], v[136:139], v[56:59]
v_add_u32 v238, 2, v238
v_xor_b32 v241, v238, v239
v_mfma_f32_16x16x32_bf16 v[76:79], a[128:131], v[136:139], v[76:79]
ds_read_b128 v[132:135], v21 offset:47104
v_and_b32 v241, 7, v241
v_mfma_f32_16x16x32_bf16 v[96:99], v[136:139], a[132:135], v[96:99]
buffer_load_dwordx4 a[116:119], v0, s[4:7], s39 offen offset:2048 sc1
v_mfma_f32_16x16x32_bf16 v[116:119], a[136:139], v[136:139], v[116:119]
v_lshl_add_u32 v241, v241, 4, v240
s_waitcnt lgkmcnt(5)
v_mfma_f32_16x16x32_bf16 v[60:63], a[124:127], v[140:143], v[60:63]
v_lshrrev_b32 v44, 3, v238
v_lshl_add_u32 v241, v44, 11, v241
v_mfma_f32_16x16x32_bf16 v[80:83], a[128:131], v[140:143], v[80:83]
ds_read_b128 v[136:139], v23 offset:6144
ds_read_b64 v[12:13], v241
v_mfma_f32_16x16x32_bf16 v[100:103], v[140:143], a[132:135], v[100:103]
buffer_load_dwordx4 a[120:123], v161, s[4:7], s39 offen offset:3072 sc1
s_add_u32 s39, s39, 0xf00
v_mfma_f32_16x16x32_bf16 v[120:123], a[136:139], v[140:143], v[120:123]
v_add_u32 v238, 2, v238
s_waitcnt lgkmcnt(6)
v_mfma_f32_16x16x32_bf16 v[64:67], a[124:127], v[144:147], v[64:67]
v_xor_b32 v241, v238, v239
v_and_b32 v241, 7, v241
v_mfma_f32_16x16x32_bf16 v[84:87], a[128:131], v[144:147], v[84:87]
ds_read_b128 v[140:143], v23 offset:26624
v_lshl_add_u32 v241, v241, 4, v240
v_mfma_f32_16x16x32_bf16 v[104:107], v[144:147], a[132:135], v[104:107]
v_mfma_f32_16x16x32_bf16 v[124:127], a[136:139], v[144:147], v[124:127]
v_lshrrev_b32 v44, 3, v238
s_waitcnt vmcnt(12) lgkmcnt(6)
v_mfma_f32_16x16x32_bf16 v[48:51], a[140:143], v[148:151], v[48:51]
v_lshl_add_u32 v241, v44, 11, v241
ds_read_b64 v[14:15], v241
v_mfma_f32_16x16x32_bf16 v[68:71], a[144:147], v[148:151], v[68:71]
ds_read_b128 v[144:147], v20 offset:8192
v_add_u32 v238, 2, v238
v_mfma_f32_16x16x32_bf16 v[88:91], v[148:151], a[148:151], v[88:91]
buffer_load_dwordx4 a[124:127], v0, s[4:7], s39 offen sc1
v_mfma_f32_16x16x32_bf16 v[108:111], a[152:155], v[148:151], v[108:111]
v_xor_b32 v241, v238, v239
s_waitcnt lgkmcnt(7)
v_mfma_f32_16x16x32_bf16 v[52:55], a[140:143], v[128:131], v[52:55]
v_and_b32 v241, 7, v241
v_lshl_add_u32 v241, v241, 4, v240
v_mfma_f32_16x16x32_bf16 v[72:75], a[144:147], v[128:131], v[72:75]
ds_read_b128 v[148:151], v20 offset:28672
v_lshrrev_b32 v44, 3, v238
v_mfma_f32_16x16x32_bf16 v[92:95], v[128:131], a[148:151], v[92:95]
buffer_load_dwordx4 a[128:131], v0, s[4:7], s39 offen offset:1024 sc1
v_mfma_f32_16x16x32_bf16 v[112:115], a[152:155], v[128:131], v[112:115]
v_lshl_add_u32 v241, v44, 11, v241
s_waitcnt lgkmcnt(6)
v_mfma_f32_16x16x32_bf16 v[56:59], a[140:143], v[132:135], v[56:59]
ds_read_b64 v[16:17], v241
v_add_u32 v238, 2, v238
v_mfma_f32_16x16x32_bf16 v[76:79], a[144:147], v[132:135], v[76:79]
ds_read_b128 v[128:131], v20 offset:49152
v_xor_b32 v241, v238, v239
v_mfma_f32_16x16x32_bf16 v[96:99], v[132:135], a[148:151], v[96:99]
buffer_load_dwordx4 a[132:135], v0, s[4:7], s39 offen offset:2048 sc1
v_mfma_f32_16x16x32_bf16 v[116:119], a[152:155], v[132:135], v[116:119]
v_and_b32 v241, 7, v241
s_waitcnt lgkmcnt(7)
v_mfma_f32_16x16x32_bf16 v[60:63], a[140:143], v[136:139], v[60:63]
v_lshl_add_u32 v241, v241, 4, v240
v_lshrrev_b32 v44, 3, v238
v_mfma_f32_16x16x32_bf16 v[80:83], a[144:147], v[136:139], v[80:83]
ds_read_b128 v[132:135], v22 offset:8192
v_lshl_add_u32 v241, v44, 11, v241
v_mfma_f32_16x16x32_bf16 v[100:103], v[136:139], a[148:151], v[100:103]
buffer_load_dwordx4 a[136:139], v161, s[4:7], s39 offen offset:3072 sc1
s_add_u32 s39, s39, 0xf00
v_mfma_f32_16x16x32_bf16 v[120:123], a[152:155], v[136:139], v[120:123]
ds_read_b64 v[18:19], v241
s_waitcnt lgkmcnt(7)
v_mfma_f32_16x16x32_bf16 v[64:67], a[140:143], v[140:143], v[64:67]
s_add_u32 s96, s36, s35
s_add_u32 s97, s38, s37
v_mfma_f32_16x16x32_bf16 v[84:87], a[144:147], v[140:143], v[84:87]
ds_read_b128 v[136:139], v22 offset:28672
s_add_u32 s43, s36, s35
v_mfma_f32_16x16x32_bf16 v[104:107], v[140:143], a[148:151], v[104:107]
v_mfma_f32_16x16x32_bf16 v[124:127], a[152:155], v[140:143], v[124:127]
s_mul_i32 s44, s43, 52
s_waitcnt vmcnt(10) lgkmcnt(6)
v_mfma_f32_16x16x32_bf16 v[48:51], a[92:95], v[144:147], v[48:51]
s_lshr_b32 s44, s44, 8
s_mul_i32 s44, s44, 5
v_mfma_f32_16x16x32_bf16 v[68:71], a[96:99], v[144:147], v[68:71]
ds_read_b128 v[140:143], v21 offset:8192
s_sub_u32 s43, s43, s44
v_mfma_f32_16x16x32_bf16 v[88:91], v[144:147], a[100:103], v[88:91]
buffer_load_dwordx4 a[140:143], v0, s[4:7], s39 offen sc1
v_mfma_f32_16x16x32_bf16 v[108:111], a[104:107], v[144:147], v[108:111]
s_add_u32 s44, s43, 0
s_waitcnt lgkmcnt(6)
v_mfma_f32_16x16x32_bf16 v[52:55], a[92:95], v[148:151], v[52:55]
s_sub_u32 s45, s44, 5
s_cmp_ge_u32 s44, 5
s_cselect_b32 s65, s45, s44
v_mfma_f32_16x16x32_bf16 v[72:75], a[96:99], v[148:151], v[72:75]
ds_read_b128 v[144:147], v21 offset:28672
s_add_u32 s44, s43, 1
v_mfma_f32_16x16x32_bf16 v[92:95], v[148:151], a[100:103], v[92:95]
buffer_load_dwordx4 a[144:147], v0, s[4:7], s39 offen offset:1024 sc1
v_mfma_f32_16x16x32_bf16 v[112:115], a[104:107], v[148:151], v[112:115]
s_sub_u32 s45, s44, 5
s_waitcnt lgkmcnt(5)
v_mfma_f32_16x16x32_bf16 v[56:59], a[92:95], v[128:131], v[56:59]
s_cmp_ge_u32 s44, 5
s_cselect_b32 s66, s45, s44
s_add_u32 s44, s43, 2
v_mfma_f32_16x16x32_bf16 v[76:79], a[96:99], v[128:131], v[76:79]
ds_read_b128 v[148:151], v21 offset:49152
s_sub_u32 s45, s44, 5
)ASM"
R"ASM(
v_mfma_f32_16x16x32_bf16 v[96:99], v[128:131], a[100:103], v[96:99]
buffer_load_dwordx4 a[148:151], v0, s[4:7], s39 offen offset:2048 sc1
v_mfma_f32_16x16x32_bf16 v[116:119], a[104:107], v[128:131], v[116:119]
s_cmp_ge_u32 s44, 5
s_cselect_b32 s67, s45, s44
s_waitcnt lgkmcnt(5)
v_mfma_f32_16x16x32_bf16 v[60:63], a[92:95], v[132:135], v[60:63]
s_add_u32 s44, s43, 3
s_sub_u32 s45, s44, 5
v_mfma_f32_16x16x32_bf16 v[80:83], a[96:99], v[132:135], v[80:83]
ds_read_b128 v[128:131], v23 offset:8192
s_cmp_ge_u32 s44, 5
s_cselect_b32 s68, s45, s44
v_mfma_f32_16x16x32_bf16 v[100:103], v[132:135], a[100:103], v[100:103]
buffer_load_dwordx4 a[152:155], v161, s[4:7], s39 offen offset:3072 sc1
s_add_u32 s39, s39, 0xf00
v_mfma_f32_16x16x32_bf16 v[120:123], a[104:107], v[132:135], v[120:123]
s_add_u32 s44, s43, 4
s_waitcnt lgkmcnt(4)
v_mfma_f32_16x16x32_bf16 v[64:67], a[92:95], v[136:139], v[64:67]
s_sub_u32 s45, s44, 5
s_cmp_ge_u32 s44, 5
s_cselect_b32 s69, s45, s44
v_mfma_f32_16x16x32_bf16 v[84:87], a[96:99], v[136:139], v[84:87]
ds_read_b128 v[132:135], v23 offset:28672
s_mul_i32 s70, s34, 0x6400
v_mfma_f32_16x16x32_bf16 v[104:107], v[136:139], a[100:103], v[104:107]
v_mfma_f32_16x16x32_bf16 v[124:127], a[104:107], v[136:139], v[124:127]
s_add_u32 s70, s70, s52
s_waitcnt vmcnt(8)
s_barrier
buffer_store_dwordx2 v[4:5], v2, s[4:7], s55 offen sc1
s_waitcnt lgkmcnt(4)
v_mfma_f32_16x16x32_bf16 v[48:51], a[108:111], v[140:143], v[48:51]
s_add_u32 s43, s97, 0
s_and_b32 s43, s43, 3
v_mfma_f32_16x16x32_bf16 v[68:71], a[112:115], v[140:143], v[68:71]
ds_read_b128 v[136:139], v20 offset:10240
s_mul_i32 s71, s43, 0x1400
v_mfma_f32_16x16x32_bf16 v[88:91], v[140:143], a[116:119], v[88:91]
buffer_load_dwordx4 a[92:95], v0, s[4:7], s39 offen sc1
v_mfma_f32_16x16x32_bf16 v[108:111], a[120:123], v[140:143], v[108:111]
s_add_u32 s43, s97, 1
s_waitcnt lgkmcnt(4)
v_mfma_f32_16x16x32_bf16 v[52:55], a[108:111], v[144:147], v[52:55]
s_and_b32 s43, s43, 3
s_mul_i32 s72, s43, 0x1400
v_mfma_f32_16x16x32_bf16 v[72:75], a[112:115], v[144:147], v[72:75]
ds_read_b128 v[140:143], v20 offset:30720
s_add_u32 s43, s97, 2
v_mfma_f32_16x16x32_bf16 v[92:95], v[144:147], a[116:119], v[92:95]
buffer_load_dwordx4 a[96:99], v0, s[4:7], s39 offen offset:1024 sc1
v_mfma_f32_16x16x32_bf16 v[112:115], a[120:123], v[144:147], v[112:115]
s_and_b32 s43, s43, 3
s_waitcnt lgkmcnt(4)
v_mfma_f32_16x16x32_bf16 v[56:59], a[108:111], v[148:151], v[56:59]
s_mul_i32 s73, s43, 0x1400
s_add_u32 s43, s97, 3
v_mfma_f32_16x16x32_bf16 v[76:79], a[112:115], v[148:151], v[76:79]
ds_read_b128 v[144:147], v20 offset:51200
s_and_b32 s43, s43, 3
v_mfma_f32_16x16x32_bf16 v[96:99], v[148:151], a[116:119], v[96:99]
buffer_load_dwordx4 a[100:103], v0, s[4:7], s39 offen offset:2048 sc1
v_mfma_f32_16x16x32_bf16 v[116:119], a[120:123], v[148:151], v[116:119]
s_mul_i32 s74, s43, 0x1400
s_waitcnt lgkmcnt(4)
v_mfma_f32_16x16x32_bf16 v[60:63], a[108:111], v[128:131], v[60:63]
s_add_u32 s43, s97, s34
s_and_b32 s43, s43, 3
v_mfma_f32_16x16x32_bf16 v[80:83], a[112:115], v[128:131], v[80:83]
ds_read_b128 v[148:151], v22 offset:10240
s_mul_i32 s43, s43, 0x1400
v_mfma_f32_16x16x32_bf16 v[100:103], v[128:131], a[116:119], v[100:103]
buffer_load_dwordx4 a[104:107], v161, s[4:7], s39 offen offset:3072 sc1
s_add_u32 s39, s39, 0xf00
v_mfma_f32_16x16x32_bf16 v[120:123], a[120:123], v[128:131], v[120:123]
s_add_u32 s60, s43, s54
s_waitcnt lgkmcnt(4)
v_mfma_f32_16x16x32_bf16 v[64:67], a[108:111], v[132:135], v[64:67]
s_mul_i32 s61, s34, 0x1400
s_add_u32 s61, s61, 0x5000
v_mfma_f32_16x16x32_bf16 v[84:87], a[112:115], v[132:135], v[84:87]
ds_read_b128 v[128:131], v22 offset:30720
s_lshl_b32 s43, s35, 2
v_mfma_f32_16x16x32_bf16 v[104:107], v[132:135], a[116:119], v[104:107]
v_mfma_f32_16x16x32_bf16 v[124:127], a[120:123], v[132:135], v[124:127]
s_add_u32 s43, s43, s34
s_waitcnt vmcnt(9) lgkmcnt(4)
v_mfma_f32_16x16x32_bf16 v[48:51], a[124:127], v[136:139], v[48:51]
s_mul_i32 s64, s43, 0x5000
s_mov_b32 s16, 0x3ea52b5c
v_mfma_f32_16x16x32_bf16 v[68:71], a[128:131], v[136:139], v[68:71]
ds_read_b128 v[132:135], v21 offset:10240
s_mul_i32 s63, s34, 0x11800
v_mfma_f32_16x16x32_bf16 v[88:91], v[136:139], a[132:135], v[88:91]
buffer_load_dwordx4 a[108:111], v0, s[4:7], s39 offen sc1
v_mfma_f32_16x16x32_bf16 v[108:111], a[136:139], v[136:139], v[108:111]
s_add_u32 s63, s63, s53
s_waitcnt lgkmcnt(4)
v_mfma_f32_16x16x32_bf16 v[52:55], a[124:127], v[140:143], v[52:55]
v_mov_b32 v242, 0xf149f2ca
v_lshlrev_b32 v238, 2, v236
v_mfma_f32_16x16x32_bf16 v[72:75], a[128:131], v[140:143], v[72:75]
ds_read_b128 v[136:139], v21 offset:30720
v_sub_u32 v238, v233, v238
v_mfma_f32_16x16x32_bf16 v[92:95], v[140:143], a[132:135], v[92:95]
buffer_load_dwordx4 a[112:115], v0, s[4:7], s39 offen offset:1024 sc1
v_mfma_f32_16x16x32_bf16 v[112:115], a[136:139], v[140:143], v[112:115]
v_subrev_u32 v239, 0, v238
s_waitcnt lgkmcnt(4)
v_mfma_f32_16x16x32_bf16 v[56:59], a[124:127], v[144:147], v[56:59]
v_and_b32 v240, 0x80000001, v239
v_cmp_eq_u32_e32 vcc, 0, v240
v_mfma_f32_16x16x32_bf16 v[76:79], a[128:131], v[144:147], v[76:79]
ds_read_b128 v[140:143], v21 offset:51200
v_add_u32 v239, 1, v239
v_mfma_f32_16x16x32_bf16 v[96:99], v[144:147], a[132:135], v[96:99]
buffer_load_dwordx4 a[116:119], v0, s[4:7], s39 offen offset:2048 sc1
v_mfma_f32_16x16x32_bf16 v[116:119], a[136:139], v[144:147], v[116:119]
v_and_b32 v240, 0x80000001, v239
s_waitcnt lgkmcnt(4)
v_mfma_f32_16x16x32_bf16 v[60:63], a[124:127], v[148:151], v[60:63]
v_cndmask_b32_e64 v28, v242, 0, vcc
v_cmp_eq_u32_e32 vcc, 0x80000001, v240
v_mfma_f32_16x16x32_bf16 v[80:83], a[128:131], v[148:151], v[80:83]
ds_read_b128 v[144:147], v23 offset:10240
s_cmp_eq_u32 s35, 0
s_cselect_b64 s[48:49], 0, vcc
v_mfma_f32_16x16x32_bf16 v[100:103], v[148:151], a[132:135], v[100:103]
buffer_load_dwordx4 a[120:123], v161, s[4:7], s39 offen offset:3072 sc1
s_add_u32 s39, s39, 0xf00
v_mfma_f32_16x16x32_bf16 v[120:123], a[136:139], v[148:151], v[120:123]
v_cndmask_b32_e64 v24, v242, 0, vcc
s_waitcnt lgkmcnt(4)
v_mfma_f32_16x16x32_bf16 v[64:67], a[124:127], v[128:131], v[64:67]
v_cndmask_b32_e64 v32, v242, 0, s[48:49]
v_subrev_u32 v239, 1, v238
v_mfma_f32_16x16x32_bf16 v[84:87], a[128:131], v[128:131], v[84:87]
ds_read_b128 v[148:151], v23 offset:30720
v_and_b32 v240, 0x80000001, v239
v_mfma_f32_16x16x32_bf16 v[104:107], v[128:131], a[132:135], v[104:107]
v_mfma_f32_16x16x32_bf16 v[124:127], a[136:139], v[128:131], v[124:127]
v_cmp_eq_u32_e32 vcc, 0, v240
s_waitcnt vmcnt(9) lgkmcnt(4)
v_mfma_f32_16x16x32_bf16 v[48:51], a[140:143], v[132:135], v[48:51]
v_add_u32 v239, 1, v239
v_and_b32 v240, 0x80000001, v239
v_mfma_f32_16x16x32_bf16 v[68:71], a[144:147], v[132:135], v[68:71]
ds_read_b128 v[128:131], v20 offset:12288
v_cndmask_b32_e64 v29, v242, 0, vcc
v_mfma_f32_16x16x32_bf16 v[88:91], v[132:135], a[148:151], v[88:91]
buffer_load_dwordx4 a[124:127], v0, s[4:7], s39 offen sc1
v_mfma_f32_16x16x32_bf16 v[108:111], a[152:155], v[132:135], v[108:111]
v_cmp_eq_u32_e32 vcc, 0x80000001, v240
s_waitcnt lgkmcnt(4)
v_mfma_f32_16x16x32_bf16 v[52:55], a[140:143], v[136:139], v[52:55]
s_cmp_eq_u32 s35, 0
s_cselect_b64 s[48:49], 0, vcc
v_cndmask_b32_e64 v25, v242, 0, vcc
v_mfma_f32_16x16x32_bf16 v[72:75], a[144:147], v[136:139], v[72:75]
ds_read_b128 v[132:135], v20 offset:32768
v_cndmask_b32_e64 v33, v242, 0, s[48:49]
v_mfma_f32_16x16x32_bf16 v[92:95], v[136:139], a[148:151], v[92:95]
buffer_load_dwordx4 a[128:131], v0, s[4:7], s39 offen offset:1024 sc1
v_mfma_f32_16x16x32_bf16 v[112:115], a[152:155], v[136:139], v[112:115]
v_subrev_u32 v239, 2, v238
s_waitcnt lgkmcnt(4)
v_mfma_f32_16x16x32_bf16 v[56:59], a[140:143], v[140:143], v[56:59]
v_and_b32 v240, 0x80000001, v239
v_cmp_eq_u32_e32 vcc, 0, v240
v_mfma_f32_16x16x32_bf16 v[76:79], a[144:147], v[140:143], v[76:79]
ds_read_b128 v[136:139], v20 offset:53248
v_add_u32 v239, 1, v239
v_mfma_f32_16x16x32_bf16 v[96:99], v[140:143], a[148:151], v[96:99]
buffer_load_dwordx4 a[132:135], v0, s[4:7], s39 offen offset:2048 sc1
v_mfma_f32_16x16x32_bf16 v[116:119], a[152:155], v[140:143], v[116:119]
v_and_b32 v240, 0x80000001, v239
s_waitcnt lgkmcnt(4)
v_mfma_f32_16x16x32_bf16 v[60:63], a[140:143], v[144:147], v[60:63]
v_cndmask_b32_e64 v30, v242, 0, vcc
v_cmp_eq_u32_e32 vcc, 0x80000001, v240
v_mfma_f32_16x16x32_bf16 v[80:83], a[144:147], v[144:147], v[80:83]
ds_read_b128 v[140:143], v22 offset:12288
s_cmp_eq_u32 s35, 0
s_cselect_b64 s[48:49], 0, vcc
v_mfma_f32_16x16x32_bf16 v[100:103], v[144:147], a[148:151], v[100:103]
buffer_load_dwordx4 a[136:139], v161, s[4:7], s39 offen offset:3072 sc1
s_add_u32 s39, s39, 0xf00
v_mfma_f32_16x16x32_bf16 v[120:123], a[152:155], v[144:147], v[120:123]
v_cndmask_b32_e64 v26, v242, 0, vcc
s_waitcnt lgkmcnt(4)
v_mfma_f32_16x16x32_bf16 v[64:67], a[140:143], v[148:151], v[64:67]
v_cndmask_b32_e64 v34, v242, 0, s[48:49]
v_subrev_u32 v239, 3, v238
v_mfma_f32_16x16x32_bf16 v[84:87], a[144:147], v[148:151], v[84:87]
ds_read_b128 v[144:147], v22 offset:32768
v_and_b32 v240, 0x80000001, v239
v_mfma_f32_16x16x32_bf16 v[104:107], v[148:151], a[148:151], v[104:107]
v_mfma_f32_16x16x32_bf16 v[124:127], a[152:155], v[148:151], v[124:127]
v_cmp_eq_u32_e32 vcc, 0, v240
)ASM"
R"ASM(
s_waitcnt vmcnt(8) lgkmcnt(4)
v_mfma_f32_16x16x32_bf16 v[48:51], a[92:95], v[128:131], v[48:51]
v_add_u32 v239, 1, v239
v_and_b32 v240, 0x80000001, v239
v_mfma_f32_16x16x32_bf16 v[68:71], a[96:99], v[128:131], v[68:71]
ds_read_b128 v[148:151], v21 offset:12288
v_cndmask_b32_e64 v31, v242, 0, vcc
v_mfma_f32_16x16x32_bf16 v[88:91], v[128:131], a[100:103], v[88:91]
buffer_load_dwordx4 a[140:143], v0, s[4:7], s39 offen sc1
v_mfma_f32_16x16x32_bf16 v[108:111], a[104:107], v[128:131], v[108:111]
v_cmp_eq_u32_e32 vcc, 0x80000001, v240
s_waitcnt lgkmcnt(4)
v_mfma_f32_16x16x32_bf16 v[52:55], a[92:95], v[132:135], v[52:55]
s_cmp_eq_u32 s35, 0
s_cselect_b64 s[48:49], 0, vcc
v_cndmask_b32_e64 v27, v242, 0, vcc
v_mfma_f32_16x16x32_bf16 v[72:75], a[96:99], v[132:135], v[72:75]
ds_read_b128 v[128:131], v21 offset:32768
v_cndmask_b32_e64 v35, v242, 0, s[48:49]
v_mfma_f32_16x16x32_bf16 v[92:95], v[132:135], a[100:103], v[92:95]
buffer_load_dwordx4 a[144:147], v0, s[4:7], s39 offen offset:1024 sc1
v_mfma_f32_16x16x32_bf16 v[112:115], a[104:107], v[132:135], v[112:115]
v_and_b32 v239, 3, v233
s_waitcnt lgkmcnt(4)
v_mfma_f32_16x16x32_bf16 v[56:59], a[92:95], v[136:139], v[56:59]
v_cmp_eq_u32_e32 vcc, 1, v239
v_lshl_or_b32 v239, v236, 2, v239
v_mfma_f32_16x16x32_bf16 v[76:79], a[96:99], v[136:139], v[76:79]
ds_read_b128 v[132:135], v21 offset:53248
v_and_b32 v240, 4, v233
v_mfma_f32_16x16x32_bf16 v[96:99], v[136:139], a[100:103], v[96:99]
buffer_load_dwordx4 a[148:151], v0, s[4:7], s39 offen offset:2048 sc1
v_mfma_f32_16x16x32_bf16 v[116:119], a[104:107], v[136:139], v[116:119]
v_cndmask_b32_e64 v36, 0, 1.0, vcc
s_waitcnt lgkmcnt(4)
v_mfma_f32_16x16x32_bf16 v[60:63], a[92:95], v[140:143], v[60:63]
v_cmp_eq_u32_e32 vcc, 8, v239
v_lshlrev_b32 v240, 2, v240
v_mfma_f32_16x16x32_bf16 v[80:83], a[96:99], v[140:143], v[80:83]
ds_read_b128 v[136:139], v23 offset:12288
v_mov_b32 v241, 0x3f80
v_mfma_f32_16x16x32_bf16 v[100:103], v[140:143], a[100:103], v[100:103]
buffer_load_dwordx4 a[152:155], v161, s[4:7], s39 offen offset:3072 sc1
s_add_u32 s39, s39, 0xf00
v_mfma_f32_16x16x32_bf16 v[120:123], a[104:107], v[140:143], v[120:123]
v_lshlrev_b32 v240, v240, v241
s_waitcnt lgkmcnt(4)
v_mfma_f32_16x16x32_bf16 v[64:67], a[92:95], v[144:147], v[64:67]
v_cndmask_b32 v240, 0, v240, vcc
v_cmp_gt_u32_e32 vcc, 8, v233
v_mfma_f32_16x16x32_bf16 v[84:87], a[96:99], v[144:147], v[84:87]
ds_read_b128 v[140:143], v23 offset:32768
v_mov_b32 v37, v36
v_mfma_f32_16x16x32_bf16 v[104:107], v[144:147], a[100:103], v[104:107]
v_mfma_f32_16x16x32_bf16 v[124:127], a[104:107], v[144:147], v[124:127]
v_mov_b32 v38, v36
s_waitcnt vmcnt(8) lgkmcnt(4)
v_mfma_f32_16x16x32_bf16 v[48:51], a[108:111], v[148:151], v[48:51]
v_mov_b32 v39, v36
v_cndmask_b32 v40, 0, v240, vcc
v_mfma_f32_16x16x32_bf16 v[68:71], a[112:115], v[148:151], v[68:71]
ds_read_b128 v[144:147], v20 offset:14336
v_cndmask_b32_e64 v41, v240, 0, vcc
v_mfma_f32_16x16x32_bf16 v[88:91], v[148:151], a[116:119], v[88:91]
buffer_load_dwordx4 a[92:95], v0, s[4:7], s39 offen sc1
v_mfma_f32_16x16x32_bf16 v[108:111], a[120:123], v[148:151], v[108:111]
s_mul_i32 s43, s34, 40
s_waitcnt lgkmcnt(4)
v_mfma_f32_16x16x32_bf16 v[52:55], a[108:111], v[128:131], v[52:55]
s_add_u32 s43, s43, 0x19000
v_mul_u32_u24 v239, 0xb0, v233
v_mfma_f32_16x16x32_bf16 v[72:75], a[112:115], v[128:131], v[72:75]
ds_read_b128 v[148:151], v20 offset:34816
v_add_u32 v240, s43, v239
v_mfma_f32_16x16x32_bf16 v[92:95], v[128:131], a[116:119], v[92:95]
buffer_load_dwordx4 a[96:99], v0, s[4:7], s39 offen offset:1024 sc1
v_mfma_f32_16x16x32_bf16 v[112:115], a[120:123], v[128:131], v[112:115]
v_lshl_add_u32 v42, v236, 3, v240
s_waitcnt lgkmcnt(4)
v_mfma_f32_16x16x32_bf16 v[56:59], a[108:111], v[132:135], v[56:59]
v_lshl_add_u32 v43, v236, 1, v240
s_mul_i32 s43, s34, 0xb00
v_mfma_f32_16x16x32_bf16 v[76:79], a[112:115], v[132:135], v[76:79]
ds_read_b128 v[128:131], v20 offset:55296
s_add_u32 s43, s43, 0x19000
v_mfma_f32_16x16x32_bf16 v[96:99], v[132:135], a[116:119], v[96:99]
buffer_load_dwordx4 a[100:103], v0, s[4:7], s39 offen offset:2048 sc1
v_mfma_f32_16x16x32_bf16 v[116:119], a[120:123], v[132:135], v[116:119]
v_lshl_add_u32 v45, v236, 4, v239
s_waitcnt lgkmcnt(4)
v_mfma_f32_16x16x32_bf16 v[60:63], a[108:111], v[136:139], v[60:63]
v_add_u32 v45, s43, v45
ds_read_b128 v[244:247], v46
v_mfma_f32_16x16x32_bf16 v[80:83], a[112:115], v[136:139], v[80:83]
ds_read_b128 v[132:135], v22 offset:14336
ds_read_b128 v[248:251], v46 offset:320
v_mfma_f32_16x16x32_bf16 v[100:103], v[136:139], a[116:119], v[100:103]
buffer_load_dwordx4 a[104:107], v161, s[4:7], s39 offen offset:3072 sc1
s_add_u32 s39, s39, 0xf00
v_mfma_f32_16x16x32_bf16 v[120:123], a[120:123], v[136:139], v[120:123]
ds_read_b128 v[228:231], v46 offset:64
s_waitcnt lgkmcnt(7)
v_mfma_f32_16x16x32_bf16 v[64:67], a[108:111], v[140:143], v[64:67]
ds_read_b128 v[232:235], v46 offset:384
ds_read_b128 v[218:221], v46 offset:128
v_mfma_f32_16x16x32_bf16 v[84:87], a[112:115], v[140:143], v[84:87]
ds_read_b128 v[136:139], v22 offset:34816
ds_read_b128 v[222:225], v46 offset:448
v_mfma_f32_16x16x32_bf16 v[104:107], v[140:143], a[116:119], v[104:107]
v_mfma_f32_16x16x32_bf16 v[124:127], a[120:123], v[140:143], v[124:127]
ds_read_b128 v[236:239], v46 offset:192
s_waitcnt vmcnt(8) lgkmcnt(11)
v_mfma_f32_16x16x32_bf16 v[48:51], a[124:127], v[144:147], v[48:51]
ds_read_b128 v[240:243], v46 offset:512
ds_read_b128 v[180:183], v46 offset:256
v_mfma_f32_16x16x32_bf16 v[68:71], a[128:131], v[144:147], v[68:71]
ds_read_b128 v[140:143], v21 offset:14336
ds_read_b128 v[184:187], v46 offset:576
v_mfma_f32_16x16x32_bf16 v[88:91], v[144:147], a[132:135], v[88:91]
buffer_load_dwordx4 a[108:111], v0, s[4:7], s39 offen sc1
v_mfma_f32_16x16x32_bf16 v[108:111], a[136:139], v[144:147], v[108:111]
s_waitcnt lgkmcnt(14)
v_mfma_f32_16x16x32_bf16 v[52:55], a[124:127], v[148:151], v[52:55]
v_mfma_f32_16x16x32_bf16 v[72:75], a[128:131], v[148:151], v[72:75]
ds_read_b128 v[144:147], v21 offset:34816
v_mfma_f32_16x16x32_bf16 v[92:95], v[148:151], a[132:135], v[92:95]
buffer_load_dwordx4 a[112:115], v0, s[4:7], s39 offen offset:1024 sc1
v_mfma_f32_16x16x32_bf16 v[112:115], a[136:139], v[148:151], v[112:115]
s_waitcnt lgkmcnt(14)
v_mfma_f32_16x16x32_bf16 v[56:59], a[124:127], v[128:131], v[56:59]
v_mfma_f32_16x16x32_bf16 v[76:79], a[128:131], v[128:131], v[76:79]
ds_read_b128 v[148:151], v21 offset:55296
v_mfma_f32_16x16x32_bf16 v[96:99], v[128:131], a[132:135], v[96:99]
buffer_load_dwordx4 a[116:119], v0, s[4:7], s39 offen offset:2048 sc1
v_mfma_f32_16x16x32_bf16 v[116:119], a[136:139], v[128:131], v[116:119]
s_waitcnt lgkmcnt(13)
v_mfma_f32_16x16x32_bf16 v[60:63], a[124:127], v[132:135], v[60:63]
v_mfma_f32_16x16x32_bf16 v[80:83], a[128:131], v[132:135], v[80:83]
ds_read_b128 v[128:131], v23 offset:14336
v_mfma_f32_16x16x32_bf16 v[100:103], v[132:135], a[132:135], v[100:103]
buffer_load_dwordx4 a[120:123], v161, s[4:7], s39 offen offset:3072 sc1
s_add_u32 s39, s39, 0xf00
v_mfma_f32_16x16x32_bf16 v[120:123], a[136:139], v[132:135], v[120:123]
s_waitcnt lgkmcnt(9)
v_mfma_f32_16x16x32_bf16 v[64:67], a[124:127], v[136:139], v[64:67]
v_mfma_f32_16x16x32_bf16 v[84:87], a[128:131], v[136:139], v[84:87]
ds_read_b128 v[132:135], v23 offset:34816
v_mfma_f32_16x16x32_bf16 v[104:107], v[136:139], a[132:135], v[104:107]
s_mov_b32 s47, 0x1000
buffer_load_dwordx2 v[226:227], v3, s[4:7], s47 offen sc1
v_mfma_f32_16x16x32_bf16 v[124:127], a[136:139], v[136:139], v[124:127]
s_waitcnt vmcnt(9) lgkmcnt(5)
v_mfma_f32_16x16x32_bf16 v[48:51], a[140:143], v[140:143], v[48:51]
v_mfma_f32_16x16x32_bf16 v[68:71], a[144:147], v[140:143], v[68:71]
ds_read_b128 v[136:139], v20 offset:16384
v_mfma_f32_16x16x32_bf16 v[88:91], v[140:143], a[148:151], v[88:91]
buffer_load_dwordx4 a[124:127], v0, s[4:7], s39 offen sc1
v_mfma_f32_16x16x32_bf16 v[108:111], a[152:155], v[140:143], v[108:111]
s_waitcnt lgkmcnt(4)
v_mfma_f32_16x16x32_bf16 v[52:55], a[140:143], v[144:147], v[52:55]
v_mfma_f32_16x16x32_bf16 v[72:75], a[144:147], v[144:147], v[72:75]
ds_read_b128 v[140:143], v20 offset:36864
v_mfma_f32_16x16x32_bf16 v[92:95], v[144:147], a[148:151], v[92:95]
buffer_load_dwordx4 a[128:131], v0, s[4:7], s39 offen offset:1024 sc1
v_mfma_f32_16x16x32_bf16 v[112:115], a[152:155], v[144:147], v[112:115]
s_waitcnt lgkmcnt(4)
v_mfma_f32_16x16x32_bf16 v[56:59], a[140:143], v[148:151], v[56:59]
v_mfma_f32_16x16x32_bf16 v[76:79], a[144:147], v[148:151], v[76:79]
ds_read_b128 v[144:147], v20 offset:57344
v_mfma_f32_16x16x32_bf16 v[96:99], v[148:151], a[148:151], v[96:99]
buffer_load_dwordx4 a[132:135], v0, s[4:7], s39 offen offset:2048 sc1
v_mfma_f32_16x16x32_bf16 v[116:119], a[152:155], v[148:151], v[116:119]
s_waitcnt lgkmcnt(4)
v_mfma_f32_16x16x32_bf16 v[60:63], a[140:143], v[128:131], v[60:63]
v_mfma_f32_16x16x32_bf16 v[80:83], a[144:147], v[128:131], v[80:83]
ds_read_b128 v[148:151], v22 offset:16384
v_mfma_f32_16x16x32_bf16 v[100:103], v[128:131], a[148:151], v[100:103]
buffer_load_dwordx4 a[136:139], v161, s[4:7], s39 offen offset:3072 sc1
s_add_u32 s39, s39, 0xf00
v_mfma_f32_16x16x32_bf16 v[120:123], a[152:155], v[128:131], v[120:123]
s_waitcnt lgkmcnt(4)
v_mfma_f32_16x16x32_bf16 v[64:67], a[140:143], v[132:135], v[64:67]
v_mfma_f32_16x16x32_bf16 v[84:87], a[144:147], v[132:135], v[84:87]
ds_read_b128 v[128:131], v22 offset:36864
v_mfma_f32_16x16x32_bf16 v[104:107], v[132:135], a[148:151], v[104:107]
v_mfma_f32_16x16x32_bf16 v[124:127], a[152:155], v[132:135], v[124:127]
s_waitcnt vmcnt(9)
s_barrier
s_waitcnt lgkmcnt(4)
v_mfma_f32_16x16x32_bf16 v[48:51], a[92:95], v[136:139], v[48:51]
v_mfma_f32_16x16x32_bf16 v[68:71], a[96:99], v[136:139], v[68:71]
ds_read_b128 v[132:135], v21 offset:16384
v_mfma_f32_16x16x32_bf16 v[88:91], v[136:139], a[100:103], v[88:91]
buffer_load_dwordx4 a[140:143], v0, s[4:7], s39 offen sc1
v_mfma_f32_16x16x32_bf16 v[108:111], a[104:107], v[136:139], v[108:111]
s_waitcnt lgkmcnt(4)
v_mfma_f32_16x16x32_bf16 v[52:55], a[92:95], v[140:143], v[52:55]
v_mfma_f32_16x16x32_bf16 v[72:75], a[96:99], v[140:143], v[72:75]
ds_read_b128 v[136:139], v21 offset:36864
v_mfma_f32_16x16x32_bf16 v[92:95], v[140:143], a[100:103], v[92:95]
)ASM"
R"ASM(
buffer_load_dwordx4 a[144:147], v0, s[4:7], s39 offen offset:1024 sc1
v_mfma_f32_16x16x32_bf16 v[112:115], a[104:107], v[140:143], v[112:115]
s_waitcnt lgkmcnt(4)
v_mfma_f32_16x16x32_bf16 v[56:59], a[92:95], v[144:147], v[56:59]
v_mfma_f32_16x16x32_bf16 v[76:79], a[96:99], v[144:147], v[76:79]
ds_read_b128 v[140:143], v21 offset:57344
v_mfma_f32_16x16x32_bf16 v[96:99], v[144:147], a[100:103], v[96:99]
buffer_load_dwordx4 a[148:151], v0, s[4:7], s39 offen offset:2048 sc1
v_mfma_f32_16x16x32_bf16 v[116:119], a[104:107], v[144:147], v[116:119]
s_waitcnt lgkmcnt(4)
v_mfma_f32_16x16x32_bf16 v[60:63], a[92:95], v[148:151], v[60:63]
v_mfma_f32_16x16x32_bf16 v[80:83], a[96:99], v[148:151], v[80:83]
ds_read_b128 v[144:147], v23 offset:16384
v_mfma_f32_16x16x32_bf16 v[100:103], v[148:151], a[100:103], v[100:103]
buffer_load_dwordx4 a[152:155], v161, s[4:7], s39 offen offset:3072 sc1
v_mfma_f32_16x16x32_bf16 v[120:123], a[104:107], v[148:151], v[120:123]
s_waitcnt lgkmcnt(4)
v_mfma_f32_16x16x32_bf16 v[64:67], a[92:95], v[128:131], v[64:67]
v_mfma_f32_16x16x32_bf16 v[84:87], a[96:99], v[128:131], v[84:87]
ds_read_b128 v[148:151], v23 offset:36864
v_mfma_f32_16x16x32_bf16 v[104:107], v[128:131], a[100:103], v[104:107]
v_mfma_f32_16x16x32_bf16 v[124:127], a[104:107], v[128:131], v[124:127]
s_waitcnt vmcnt(8)
v_cmp_eq_u64_e64 s[94:95], s[40:41], v[226:227]
s_nop 0
s_cmp_eq_u64 s[94:95], exec
s_cbranch_scc0 .Lk_fls_4
.Lk_flb_5:
s_waitcnt lgkmcnt(4)
v_mfma_f32_16x16x32_bf16 v[48:51], a[108:111], v[132:135], v[48:51]
v_mfma_f32_16x16x32_bf16 v[68:71], a[112:115], v[132:135], v[68:71]
ds_read_b128 v[128:131], v20 offset:18432
v_mfma_f32_16x16x32_bf16 v[88:91], v[132:135], a[116:119], v[88:91]
s_mul_i32 s46, s65, 0x1400
s_add_u32 s46, s46, s70
buffer_load_dwordx4 a[156:159], v0, s[4:7], s46 offen sc1
v_mfma_f32_16x16x32_bf16 v[108:111], a[120:123], v[132:135], v[108:111]
s_waitcnt lgkmcnt(4)
v_mfma_f32_16x16x32_bf16 v[52:55], a[108:111], v[136:139], v[52:55]
v_mfma_f32_16x16x32_bf16 v[72:75], a[112:115], v[136:139], v[72:75]
ds_read_b128 v[132:135], v20 offset:38912
v_mfma_f32_16x16x32_bf16 v[92:95], v[136:139], a[116:119], v[92:95]
buffer_load_dwordx4 a[160:163], v0, s[4:7], s46 offen offset:1024 sc1
v_mfma_f32_16x16x32_bf16 v[112:115], a[120:123], v[136:139], v[112:115]
s_waitcnt lgkmcnt(4)
v_mfma_f32_16x16x32_bf16 v[56:59], a[108:111], v[140:143], v[56:59]
v_mfma_f32_16x16x32_bf16 v[76:79], a[112:115], v[140:143], v[76:79]
ds_read_b128 v[136:139], v20 offset:59392
v_mfma_f32_16x16x32_bf16 v[96:99], v[140:143], a[116:119], v[96:99]
buffer_load_dwordx4 a[164:167], v0, s[4:7], s46 offen offset:2048 sc1
v_mfma_f32_16x16x32_bf16 v[116:119], a[120:123], v[140:143], v[116:119]
s_waitcnt lgkmcnt(4)
v_mfma_f32_16x16x32_bf16 v[60:63], a[108:111], v[144:147], v[60:63]
v_mfma_f32_16x16x32_bf16 v[80:83], a[112:115], v[144:147], v[80:83]
ds_read_b128 v[140:143], v22 offset:18432
v_mfma_f32_16x16x32_bf16 v[100:103], v[144:147], a[116:119], v[100:103]
buffer_load_dwordx4 a[168:171], v0, s[4:7], s46 offen offset:3072 sc1
v_mfma_f32_16x16x32_bf16 v[120:123], a[120:123], v[144:147], v[120:123]
s_waitcnt lgkmcnt(4)
v_mfma_f32_16x16x32_bf16 v[64:67], a[108:111], v[148:151], v[64:67]
v_mfma_f32_16x16x32_bf16 v[84:87], a[112:115], v[148:151], v[84:87]
ds_read_b128 v[144:147], v22 offset:38912
v_mfma_f32_16x16x32_bf16 v[104:107], v[148:151], a[116:119], v[104:107]
v_mfma_f32_16x16x32_bf16 v[124:127], a[120:123], v[148:151], v[124:127]
s_waitcnt vmcnt(8) lgkmcnt(4)
v_mfma_f32_16x16x32_bf16 v[48:51], a[124:127], v[128:131], v[48:51]
v_mfma_f32_16x16x32_bf16 v[68:71], a[128:131], v[128:131], v[68:71]
ds_read_b128 v[148:151], v21 offset:18432
v_mfma_f32_16x16x32_bf16 v[88:91], v[128:131], a[132:135], v[88:91]
s_add_u32 s46, s46, 0x1000
buffer_load_dwordx4 a[172:175], v0, s[4:7], s46 offen sc1
v_mfma_f32_16x16x32_bf16 v[108:111], a[136:139], v[128:131], v[108:111]
v_mov_b32 v190, 0
v_mov_b32 v191, 0
s_waitcnt lgkmcnt(4)
v_mfma_f32_16x16x32_bf16 v[52:55], a[124:127], v[132:135], v[52:55]
v_mfma_f32_16x16x32_bf16 v[72:75], a[128:131], v[132:135], v[72:75]
ds_read_b128 v[128:131], v21 offset:38912
v_mfma_f32_16x16x32_bf16 v[92:95], v[132:135], a[132:135], v[92:95]
s_mul_i32 s46, s66, 0x1400
s_add_u32 s46, s46, s70
buffer_load_dwordx4 a[176:179], v0, s[4:7], s46 offen sc1
v_mfma_f32_16x16x32_bf16 v[112:115], a[136:139], v[132:135], v[112:115]
v_mov_b32 v194, 0
v_mov_b32 v195, 0
s_waitcnt lgkmcnt(4)
v_mfma_f32_16x16x32_bf16 v[56:59], a[124:127], v[136:139], v[56:59]
v_mfma_f32_16x16x32_bf16 v[76:79], a[128:131], v[136:139], v[76:79]
ds_read_b128 v[132:135], v21 offset:59392
v_mfma_f32_16x16x32_bf16 v[96:99], v[136:139], a[132:135], v[96:99]
buffer_load_dwordx4 a[180:183], v0, s[4:7], s46 offen offset:1024 sc1
v_mfma_f32_16x16x32_bf16 v[116:119], a[136:139], v[136:139], v[116:119]
v_mov_b32 v198, 0
v_mov_b32 v199, 0
s_waitcnt lgkmcnt(4)
v_mfma_f32_16x16x32_bf16 v[60:63], a[124:127], v[140:143], v[60:63]
v_mfma_f32_16x16x32_bf16 v[80:83], a[128:131], v[140:143], v[80:83]
ds_read_b128 v[136:139], v23 offset:18432
v_mfma_f32_16x16x32_bf16 v[100:103], v[140:143], a[132:135], v[100:103]
buffer_load_dwordx4 a[184:187], v0, s[4:7], s46 offen offset:2048 sc1
v_mfma_f32_16x16x32_bf16 v[120:123], a[136:139], v[140:143], v[120:123]
v_mov_b32 v202, 0
v_mov_b32 v203, 0
s_waitcnt lgkmcnt(4)
v_mfma_f32_16x16x32_bf16 v[64:67], a[124:127], v[144:147], v[64:67]
v_mfma_f32_16x16x32_bf16 v[84:87], a[128:131], v[144:147], v[84:87]
ds_read_b128 v[140:143], v23 offset:38912
v_mfma_f32_16x16x32_bf16 v[104:107], v[144:147], a[132:135], v[104:107]
v_mfma_f32_16x16x32_bf16 v[124:127], a[136:139], v[144:147], v[124:127]
v_mov_b32 v206, 0
v_mov_b32 v207, 0
s_waitcnt vmcnt(8) lgkmcnt(4)
v_mfma_f32_16x16x32_bf16 v[48:51], a[140:143], v[148:151], v[48:51]
v_mfma_f32_16x16x32_bf16 v[68:71], a[144:147], v[148:151], v[68:71]
v_mfma_f32_16x16x32_bf16 v[88:91], v[148:151], a[148:151], v[88:91]
buffer_load_dwordx4 a[188:191], v0, s[4:7], s46 offen offset:3072 sc1
v_mfma_f32_16x16x32_bf16 v[108:111], a[152:155], v[148:151], v[108:111]
s_waitcnt lgkmcnt(3)
v_mfma_f32_16x16x32_bf16 v[52:55], a[140:143], v[128:131], v[52:55]
v_mfma_f32_16x16x32_bf16 v[72:75], a[144:147], v[128:131], v[72:75]
v_mfma_f32_16x16x32_bf16 v[92:95], v[128:131], a[148:151], v[92:95]
s_add_u32 s46, s46, 0x1000
buffer_load_dwordx4 a[192:195], v0, s[4:7], s46 offen sc1
v_mfma_f32_16x16x32_bf16 v[112:115], a[152:155], v[128:131], v[112:115]
s_waitcnt lgkmcnt(2)
v_mfma_f32_16x16x32_bf16 v[56:59], a[140:143], v[132:135], v[56:59]
v_fmac_f32_dpp v68, v68, v152 row_newbcast:0 row_mask:0xf bank_mask:0xf
v_fmac_f32_dpp v69, v69, v152 row_newbcast:0 row_mask:0xf bank_mask:0xf
v_mfma_f32_16x16x32_bf16 v[76:79], a[144:147], v[132:135], v[76:79]
v_fmac_f32_dpp v70, v70, v152 row_newbcast:0 row_mask:0xf bank_mask:0xf
v_fmac_f32_dpp v71, v71, v152 row_newbcast:0 row_mask:0xf bank_mask:0xf
v_mfma_f32_16x16x32_bf16 v[96:99], v[132:135], a[148:151], v[96:99]
s_mul_i32 s46, s67, 0x1400
s_add_u32 s46, s46, s70
buffer_load_dwordx4 a[196:199], v0, s[4:7], s46 offen sc1
v_mfma_f32_16x16x32_bf16 v[116:119], a[152:155], v[132:135], v[116:119]
v_fmac_f32_dpp v108, v108, v152 row_newbcast:0 row_mask:0xf bank_mask:0xf
v_fmac_f32_dpp v109, v109, v152 row_newbcast:0 row_mask:0xf bank_mask:0xf
s_waitcnt lgkmcnt(1)
v_mfma_f32_16x16x32_bf16 v[60:63], a[140:143], v[136:139], v[60:63]
v_fmac_f32_dpp v110, v110, v152 row_newbcast:0 row_mask:0xf bank_mask:0xf
v_fmac_f32_dpp v111, v111, v152 row_newbcast:0 row_mask:0xf bank_mask:0xf
v_mfma_f32_16x16x32_bf16 v[80:83], a[144:147], v[136:139], v[80:83]
v_fmac_f32_dpp v52, v48, v154 row_newbcast:0 row_mask:0xf bank_mask:0xf
v_fmac_f32_dpp v53, v49, v154 row_newbcast:0 row_mask:0xf bank_mask:0xf
v_mfma_f32_16x16x32_bf16 v[100:103], v[136:139], a[148:151], v[100:103]
buffer_load_dwordx4 a[200:203], v0, s[4:7], s46 offen offset:1024 sc1
v_mfma_f32_16x16x32_bf16 v[120:123], a[152:155], v[136:139], v[120:123]
v_fmac_f32_dpp v54, v50, v154 row_newbcast:0 row_mask:0xf bank_mask:0xf
v_fmac_f32_dpp v55, v51, v154 row_newbcast:0 row_mask:0xf bank_mask:0xf
s_waitcnt lgkmcnt(0)
v_mfma_f32_16x16x32_bf16 v[64:67], a[140:143], v[140:143], v[64:67]
v_fmac_f32_dpp v72, v68, v154 row_newbcast:0 row_mask:0xf bank_mask:0xf
v_fmac_f32_dpp v73, v69, v154 row_newbcast:0 row_mask:0xf bank_mask:0xf
v_mfma_f32_16x16x32_bf16 v[84:87], a[144:147], v[140:143], v[84:87]
v_fmac_f32_dpp v74, v70, v154 row_newbcast:0 row_mask:0xf bank_mask:0xf
v_fmac_f32_dpp v75, v71, v154 row_newbcast:0 row_mask:0xf bank_mask:0xf
v_mfma_f32_16x16x32_bf16 v[104:107], v[140:143], a[148:151], v[104:107]
v_mfma_f32_16x16x32_bf16 v[124:127], a[152:155], v[140:143], v[124:127]
v_fmac_f32_dpp v112, v108, v154 row_newbcast:0 row_mask:0xf bank_mask:0xf
v_fmac_f32_dpp v113, v109, v154 row_newbcast:0 row_mask:0xf bank_mask:0xf
v_fmac_f32_dpp v114, v110, v154 row_newbcast:0 row_mask:0xf bank_mask:0xf
v_fmac_f32_dpp v115, v111, v154 row_newbcast:0 row_mask:0xf bank_mask:0xf
v_mov_b32 v46, v88
buffer_load_dwordx4 a[204:207], v0, s[4:7], s46 offen offset:2048 sc1
v_mov_b32 v47, v88
s_nop 1
v_permlane16_swap_b32 v46, v47
v_mov_b32 v47, v46
s_nop 1
v_permlane32_swap_b32 v46, v47
v_pk_mul_f32 v[68:69], v[68:69], v[152:153] op_sel:[0,1] op_sel_hi:[1,1]
v_pk_mul_f32 v[70:71], v[70:71], v[152:153] op_sel:[0,1] op_sel_hi:[1,1]
v_pk_mul_f32 v[108:109], v[108:109], v[152:153] op_sel:[0,1] op_sel_hi:[1,1]
v_pk_mul_f32 v[110:111], v[110:111], v[152:153] op_sel:[0,1] op_sel_hi:[1,1]
v_pk_fma_f32 v[88:89], v[244:245], v[46:47], v[88:89] op_sel_hi:[1,0,1]
v_pk_fma_f32 v[90:91], v[246:247], v[46:47], v[90:91] op_sel_hi:[1,0,1]
v_pk_mul_f32 v[88:89], v[88:89], v[248:249]
v_pk_mul_f32 v[90:91], v[90:91], v[250:251]
v_cvt_pk_bf16_f32 v170, v108, v109
buffer_load_dwordx4 a[208:211], v0, s[4:7], s46 offen offset:3072 sc1
v_cvt_pk_bf16_f32 v171, v110, v111
v_cvt_pk_bf16_f32 v188, v68, v69
v_cvt_pk_bf16_f32 v189, v70, v71
v_permlane16_swap_b32 v170, v190
v_permlane16_swap_b32 v171, v191
v_cvt_pk_bf16_f32 v208, v88, v89
v_cvt_pk_bf16_f32 v209, v90, v91
v_mfma_f32_16x16x16_bf16 v[128:131], v[170:171], v[40:41], v[36:39]
v_pk_mul_f32 v[52:53], v[52:53], v[154:155] op_sel:[0,1] op_sel_hi:[1,1]
v_pk_mul_f32 v[54:55], v[54:55], v[154:155] op_sel:[0,1] op_sel_hi:[1,1]
v_pk_mul_f32 v[72:73], v[72:73], v[154:155] op_sel:[0,1] op_sel_hi:[1,1]
v_pk_mul_f32 v[74:75], v[74:75], v[154:155] op_sel:[0,1] op_sel_hi:[1,1]
s_add_u32 s46, s46, 0x1000
buffer_load_dwordx4 a[212:215], v0, s[4:7], s46 offen sc1
v_pk_mul_f32 v[112:113], v[112:113], v[154:155] op_sel:[0,1] op_sel_hi:[1,1]
v_pk_mul_f32 v[114:115], v[114:115], v[154:155] op_sel:[0,1] op_sel_hi:[1,1]
v_pk_fma_f32 v[92:93], v[228:229], v[46:47], v[92:93] op_sel_hi:[1,0,1]
v_pk_fma_f32 v[94:95], v[230:231], v[46:47], v[94:95] op_sel_hi:[1,0,1]
v_pk_mul_f32 v[92:93], v[92:93], v[232:233]
)ASM"
R"ASM(
v_pk_mul_f32 v[94:95], v[94:95], v[234:235]
v_cvt_pk_bf16_f32 v174, v112, v113
v_cvt_pk_bf16_f32 v175, v114, v115
v_cvt_pk_bf16_f32 v192, v72, v73
v_cvt_pk_bf16_f32 v193, v74, v75
v_permlane16_swap_b32 v174, v194
v_permlane16_swap_b32 v175, v195
v_cvt_pk_bf16_f32 v172, v52, v53
s_mul_i32 s46, s68, 0x1400
s_add_u32 s46, s46, s70
buffer_load_dwordx4 a[216:219], v0, s[4:7], s46 offen sc1
v_cvt_pk_bf16_f32 v173, v54, v55
v_cvt_pk_bf16_f32 v210, v92, v93
v_cvt_pk_bf16_f32 v211, v94, v95
v_mfma_f32_16x16x16_bf16 v[132:135], v[174:175], v[40:41], v[36:39]
v_mfma_f32_16x16x32_bf16 v[148:151], v[188:191], v[172:175], v[32:35]
v_mfma_f32_16x16x32_bf16 v[152:155], v[192:195], v[172:175], v[28:31]
v_fmac_f32_dpp v56, v48, v162 row_newbcast:0 row_mask:0xf bank_mask:0xf
v_fmac_f32_dpp v57, v49, v162 row_newbcast:0 row_mask:0xf bank_mask:0xf
v_fmac_f32_dpp v58, v50, v162 row_newbcast:0 row_mask:0xf bank_mask:0xf
v_fmac_f32_dpp v59, v51, v162 row_newbcast:0 row_mask:0xf bank_mask:0xf
v_fmac_f32_dpp v76, v68, v162 row_newbcast:0 row_mask:0xf bank_mask:0xf
v_fmac_f32_dpp v77, v69, v162 row_newbcast:0 row_mask:0xf bank_mask:0xf
buffer_load_dwordx4 a[220:223], v0, s[4:7], s46 offen offset:1024 sc1
v_fmac_f32_dpp v78, v70, v162 row_newbcast:0 row_mask:0xf bank_mask:0xf
v_fmac_f32_dpp v79, v71, v162 row_newbcast:0 row_mask:0xf bank_mask:0xf
v_fmac_f32_dpp v116, v108, v162 row_newbcast:0 row_mask:0xf bank_mask:0xf
v_fmac_f32_dpp v117, v109, v162 row_newbcast:0 row_mask:0xf bank_mask:0xf
v_fmac_f32_dpp v118, v110, v162 row_newbcast:0 row_mask:0xf bank_mask:0xf
v_fmac_f32_dpp v119, v111, v162 row_newbcast:0 row_mask:0xf bank_mask:0xf
v_pk_mul_f32 v[56:57], v[56:57], v[162:163] op_sel:[0,1] op_sel_hi:[1,1]
v_pk_mul_f32 v[58:59], v[58:59], v[162:163] op_sel:[0,1] op_sel_hi:[1,1]
v_pk_mul_f32 v[76:77], v[76:77], v[162:163] op_sel:[0,1] op_sel_hi:[1,1]
v_pk_mul_f32 v[78:79], v[78:79], v[162:163] op_sel:[0,1] op_sel_hi:[1,1]
v_pk_mul_f32 v[116:117], v[116:117], v[162:163] op_sel:[0,1] op_sel_hi:[1,1]
v_pk_mul_f32 v[118:119], v[118:119], v[162:163] op_sel:[0,1] op_sel_hi:[1,1]
v_pk_fma_f32 v[96:97], v[218:219], v[46:47], v[96:97] op_sel_hi:[1,0,1]
buffer_load_dwordx4 a[224:227], v0, s[4:7], s46 offen offset:2048 sc1
v_pk_fma_f32 v[98:99], v[220:221], v[46:47], v[98:99] op_sel_hi:[1,0,1]
v_pk_mul_f32 v[96:97], v[96:97], v[222:223]
v_pk_mul_f32 v[98:99], v[98:99], v[224:225]
v_pk_fma_f32 v[104:105], v[180:181], v[46:47], v[104:105] op_sel_hi:[1,0,1]
v_pk_fma_f32 v[106:107], v[182:183], v[46:47], v[106:107] op_sel_hi:[1,0,1]
v_pk_mul_f32 v[104:105], v[104:105], v[184:185]
v_pk_mul_f32 v[106:107], v[106:107], v[186:187]
v_cvt_pk_bf16_f32 v178, v116, v117
v_cvt_pk_bf16_f32 v179, v118, v119
v_cvt_pk_bf16_f32 v196, v76, v77
v_cvt_pk_bf16_f32 v197, v78, v79
v_permlane16_swap_b32 v178, v198
buffer_load_dwordx4 a[228:231], v0, s[4:7], s46 offen offset:3072 sc1
v_permlane16_swap_b32 v179, v199
v_cvt_pk_bf16_f32 v176, v56, v57
v_cvt_pk_bf16_f32 v177, v58, v59
v_cvt_pk_bf16_f32 v212, v96, v97
v_cvt_pk_bf16_f32 v213, v98, v99
v_mfma_f32_16x16x16_bf16 v[136:139], v[178:179], v[40:41], v[36:39]
v_mfma_f32_16x16x32_bf16 v[156:159], v[192:195], v[176:179], v[24:27]
v_mfma_f32_16x16x32_bf16 v[160:163], v[196:199], v[176:179], v[28:31]
v_fmac_f32_dpp v60, v48, v164 row_newbcast:0 row_mask:0xf bank_mask:0xf
v_fmac_f32_dpp v61, v49, v164 row_newbcast:0 row_mask:0xf bank_mask:0xf
v_fmac_f32_dpp v62, v50, v164 row_newbcast:0 row_mask:0xf bank_mask:0xf
v_fmac_f32_dpp v63, v51, v164 row_newbcast:0 row_mask:0xf bank_mask:0xf
v_fmac_f32_dpp v80, v68, v164 row_newbcast:0 row_mask:0xf bank_mask:0xf
s_add_u32 s46, s46, 0x1000
buffer_load_dwordx4 a[232:235], v0, s[4:7], s46 offen sc1
v_fmac_f32_dpp v81, v69, v164 row_newbcast:0 row_mask:0xf bank_mask:0xf
v_fmac_f32_dpp v82, v70, v164 row_newbcast:0 row_mask:0xf bank_mask:0xf
v_fmac_f32_dpp v83, v71, v164 row_newbcast:0 row_mask:0xf bank_mask:0xf
v_fmac_f32_dpp v120, v108, v164 row_newbcast:0 row_mask:0xf bank_mask:0xf
v_fmac_f32_dpp v121, v109, v164 row_newbcast:0 row_mask:0xf bank_mask:0xf
v_fmac_f32_dpp v122, v110, v164 row_newbcast:0 row_mask:0xf bank_mask:0xf
v_fmac_f32_dpp v123, v111, v164 row_newbcast:0 row_mask:0xf bank_mask:0xf
v_pk_mul_f32 v[60:61], v[60:61], v[164:165] op_sel:[0,1] op_sel_hi:[1,1]
v_pk_mul_f32 v[62:63], v[62:63], v[164:165] op_sel:[0,1] op_sel_hi:[1,1]
v_pk_mul_f32 v[80:81], v[80:81], v[164:165] op_sel:[0,1] op_sel_hi:[1,1]
v_pk_mul_f32 v[82:83], v[82:83], v[164:165] op_sel:[0,1] op_sel_hi:[1,1]
v_pk_mul_f32 v[120:121], v[120:121], v[164:165] op_sel:[0,1] op_sel_hi:[1,1]
s_mul_i32 s46, s69, 0x1400
s_add_u32 s46, s46, s70
buffer_load_dwordx4 a[236:239], v0, s[4:7], s46 offen sc1
v_pk_mul_f32 v[122:123], v[122:123], v[164:165] op_sel:[0,1] op_sel_hi:[1,1]
v_pk_fma_f32 v[100:101], v[236:237], v[46:47], v[100:101] op_sel_hi:[1,0,1]
v_pk_fma_f32 v[102:103], v[238:239], v[46:47], v[102:103] op_sel_hi:[1,0,1]
v_pk_mul_f32 v[100:101], v[100:101], v[240:241]
v_pk_mul_f32 v[102:103], v[102:103], v[242:243]
v_cvt_pk_bf16_f32 v182, v120, v121
v_cvt_pk_bf16_f32 v183, v122, v123
v_cvt_pk_bf16_f32 v200, v80, v81
v_cvt_pk_bf16_f32 v201, v82, v83
v_permlane16_swap_b32 v182, v202
v_permlane16_swap_b32 v183, v203
v_cvt_pk_bf16_f32 v180, v60, v61
v_cvt_pk_bf16_f32 v181, v62, v63
buffer_load_dwordx4 a[240:243], v0, s[4:7], s46 offen offset:1024 sc1
v_cvt_pk_bf16_f32 v214, v100, v101
v_cvt_pk_bf16_f32 v215, v102, v103
v_mfma_f32_16x16x16_bf16 v[140:143], v[182:183], v[40:41], v[36:39]
v_mfma_f32_16x16x32_bf16 v[228:231], v[196:199], v[180:183], v[24:27]
v_mfma_f32_16x16x32_bf16 v[232:235], v[200:203], v[180:183], v[28:31]
v_fmac_f32_dpp v64, v48, v166 row_newbcast:0 row_mask:0xf bank_mask:0xf
v_fmac_f32_dpp v65, v49, v166 row_newbcast:0 row_mask:0xf bank_mask:0xf
v_fmac_f32_dpp v66, v50, v166 row_newbcast:0 row_mask:0xf bank_mask:0xf
v_fmac_f32_dpp v67, v51, v166 row_newbcast:0 row_mask:0xf bank_mask:0xf
v_fmac_f32_dpp v84, v68, v166 row_newbcast:0 row_mask:0xf bank_mask:0xf
v_fmac_f32_dpp v85, v69, v166 row_newbcast:0 row_mask:0xf bank_mask:0xf
v_fmac_f32_dpp v86, v70, v166 row_newbcast:0 row_mask:0xf bank_mask:0xf
buffer_load_dwordx4 a[244:247], v0, s[4:7], s46 offen offset:2048 sc1
v_fmac_f32_dpp v87, v71, v166 row_newbcast:0 row_mask:0xf bank_mask:0xf
v_fmac_f32_dpp v124, v108, v166 row_newbcast:0 row_mask:0xf bank_mask:0xf
v_fmac_f32_dpp v125, v109, v166 row_newbcast:0 row_mask:0xf bank_mask:0xf
v_fmac_f32_dpp v126, v110, v166 row_newbcast:0 row_mask:0xf bank_mask:0xf
v_fmac_f32_dpp v127, v111, v166 row_newbcast:0 row_mask:0xf bank_mask:0xf
v_pk_mul_f32 v[64:65], v[64:65], v[166:167] op_sel:[0,1] op_sel_hi:[1,1]
v_pk_mul_f32 v[66:67], v[66:67], v[166:167] op_sel:[0,1] op_sel_hi:[1,1]
v_pk_mul_f32 v[84:85], v[84:85], v[166:167] op_sel:[0,1] op_sel_hi:[1,1]
v_pk_mul_f32 v[86:87], v[86:87], v[166:167] op_sel:[0,1] op_sel_hi:[1,1]
v_pk_mul_f32 v[124:125], v[124:125], v[166:167] op_sel:[0,1] op_sel_hi:[1,1]
v_pk_mul_f32 v[126:127], v[126:127], v[166:167] op_sel:[0,1] op_sel_hi:[1,1]
v_cvt_pk_bf16_f32 v186, v124, v125
v_cvt_pk_bf16_f32 v187, v126, v127
buffer_load_dwordx4 a[248:251], v0, s[4:7], s46 offen offset:3072 sc1
v_cvt_pk_bf16_f32 v204, v84, v85
v_cvt_pk_bf16_f32 v205, v86, v87
v_permlane16_swap_b32 v186, v206
v_permlane16_swap_b32 v187, v207
v_cvt_pk_bf16_f32 v184, v64, v65
v_cvt_pk_bf16_f32 v185, v66, v67
v_cvt_pk_bf16_f32 v216, v104, v105
v_cvt_pk_bf16_f32 v217, v106, v107
v_mfma_f32_16x16x16_bf16 v[144:147], v[186:187], v[40:41], v[36:39]
v_mfma_f32_16x16x32_bf16 v[236:239], v[200:203], v[184:187], v[24:27]
v_mfma_f32_16x16x32_bf16 v[240:243], v[204:207], v[184:187], v[28:31]
v_cvt_pk_bf16_f32 v218, v128, v129
s_add_u32 s46, s46, 0x1000
buffer_load_dwordx4 a[252:255], v0, s[4:7], s46 offen sc1
v_cvt_pk_bf16_f32 v219, v130, v131
v_cvt_pk_bf16_f32 v220, v132, v133
v_cvt_pk_bf16_f32 v221, v134, v135
v_cvt_pk_bf16_f32 v222, v136, v137
v_cvt_pk_bf16_f32 v223, v138, v139
v_maximum3_f32 v60, v148, v149, v150
v_maximum3_f32 v62, v151, v152, v153
v_maximum3_f32 v60, v60, v154, v155
v_maximum3_f32 v60, v60, v62, v62
v_maximum3_f32 v46, v156, v157, v158
v_maximum3_f32 v48, v159, v160, v161
v_maximum3_f32 v46, v46, v162, v163
v_maximum3_f32 v46, v46, v48, v48
s_add_u32 s47, s60, 0x1e000
s_add_u32 m0, s61, 0x1e000
s_nop 0
buffer_load_dwordx4 v0, s[4:7], s47 offen sc1 lds
v_mov_b32 v62, v60
v_mov_b32 v48, v46
s_nop 0
v_permlane32_swap_b32 v60, v62
v_permlane32_swap_b32 v46, v48
v_maximum3_f32 v60, v60, v62, v62
v_maximum3_f32 v46, v46, v48, v48
v_mov_b32 v62, v60
v_mov_b32 v48, v46
s_nop 0
v_permlane16_swap_b32 v60, v62
v_permlane16_swap_b32 v46, v48
v_maximum3_f32 v60, v60, v62, v62
v_maximum3_f32 v46, v46, v48, v48
buffer_load_dwordx4 v0, s[4:7], s47 offen offset:1024 sc1 lds
v_mul_f32_e64 v60, -v60, s16
v_pk_fma_f32 v[148:149], v[148:149], s[16:17], v[60:61] op_sel_hi:[1,0,0]
v_pk_fma_f32 v[150:151], v[150:151], s[16:17], v[60:61] op_sel_hi:[1,0,0]
v_pk_fma_f32 v[152:153], v[152:153], s[16:17], v[60:61] op_sel_hi:[1,0,0]
v_pk_fma_f32 v[154:155], v[154:155], s[16:17], v[60:61] op_sel_hi:[1,0,0]
v_exp_f32 v148, v148
v_exp_f32 v149, v149
v_exp_f32 v150, v150
v_exp_f32 v151, v151
v_exp_f32 v152, v152
v_exp_f32 v153, v153
v_exp_f32 v154, v154
v_exp_f32 v155, v155
buffer_load_dwordx4 v0, s[4:7], s47 offen offset:2048 sc1 lds
v_cvt_pk_bf16_f32 v148, v148, v149
v_cvt_pk_bf16_f32 v149, v150, v151
v_cvt_pk_bf16_f32 v150, v152, v153
v_cvt_pk_bf16_f32 v151, v154, v155
v_mul_f32_e64 v46, -v46, s16
v_pk_fma_f32 v[156:157], v[156:157], s[16:17], v[46:47] op_sel_hi:[1,0,0]
v_pk_fma_f32 v[158:159], v[158:159], s[16:17], v[46:47] op_sel_hi:[1,0,0]
v_pk_fma_f32 v[160:161], v[160:161], s[16:17], v[46:47] op_sel_hi:[1,0,0]
v_pk_fma_f32 v[162:163], v[162:163], s[16:17], v[46:47] op_sel_hi:[1,0,0]
v_exp_f32 v156, v156
v_exp_f32 v157, v157
v_exp_f32 v158, v158
buffer_load_dwordx4 v0, s[4:7], s47 offen offset:3072 sc1 lds
v_exp_f32 v159, v159
v_exp_f32 v160, v160
v_exp_f32 v161, v161
v_exp_f32 v162, v162
v_exp_f32 v163, v163
)ASM"
R"ASM(
v_cvt_pk_bf16_f32 v156, v156, v157
v_cvt_pk_bf16_f32 v157, v158, v159
v_cvt_pk_bf16_f32 v158, v160, v161
v_cvt_pk_bf16_f32 v159, v162, v163
v_mfma_f32_16x16x32_bf16 v[56:59], v[218:221], v[148:151], 0
v_mfma_f32_16x16x32_bf16 v[52:55], v[208:211], v[148:151], 0
v_mfma_f32_16x16x32_bf16 v[248:251], v[220:223], v[156:159], 0
v_mfma_f32_16x16x32_bf16 v[244:247], v[210:213], v[156:159], 0
s_add_u32 m0, m0, 0x1000
s_add_u32 s47, s47, 0x1000
buffer_load_dwordx4 v0, s[4:7], s47 offen sc1 lds
v_cvt_pk_bf16_f32 v224, v140, v141
v_cvt_pk_bf16_f32 v225, v142, v143
v_cvt_pk_bf16_f32 v226, v144, v145
v_cvt_pk_bf16_f32 v227, v146, v147
v_maximum3_f32 v60, v228, v229, v230
v_maximum3_f32 v62, v231, v232, v233
v_maximum3_f32 v60, v60, v234, v235
v_maximum3_f32 v60, v60, v62, v62
v_maximum3_f32 v46, v236, v237, v238
v_maximum3_f32 v48, v239, v240, v241
v_maximum3_f32 v46, v46, v242, v243
v_maximum3_f32 v46, v46, v48, v48
s_add_u32 s47, s60, 0x19000
s_add_u32 m0, s61, 0x19000
s_nop 0
buffer_load_dwordx4 v0, s[4:7], s47 offen sc1 lds
v_rcp_f32 v58, v57
v_rcp_f32 v250, v249
v_pk_mul_f32 v[52:53], v[52:53], v[58:59] op_sel_hi:[1,0]
v_pk_mul_f32 v[54:55], v[54:55], v[58:59] op_sel_hi:[1,0]
v_mul_f32 v56, v56, v58
v_cvt_pk_bf16_f32 v52, v52, v53
v_cvt_pk_bf16_f32 v53, v54, v55
v_cvt_pk_bf16_f32 v56, v56, v56
ds_write_b64 v42, v[52:53]
ds_write_b16 v43, v56 offset:32
v_pk_mul_f32 v[244:245], v[244:245], v[250:251] op_sel_hi:[1,0]
v_pk_mul_f32 v[246:247], v[246:247], v[250:251] op_sel_hi:[1,0]
v_mul_f32 v248, v248, v250
buffer_load_dwordx4 v0, s[4:7], s47 offen offset:1024 sc1 lds
v_cvt_pk_bf16_f32 v244, v244, v245
v_cvt_pk_bf16_f32 v245, v246, v247
v_cvt_pk_bf16_f32 v248, v248, v248
ds_write_b64 v42, v[244:245] offset:2816
ds_write_b16 v43, v248 offset:2848
v_mov_b32 v62, v60
v_mov_b32 v48, v46
s_nop 0
v_permlane32_swap_b32 v60, v62
v_permlane32_swap_b32 v46, v48
v_maximum3_f32 v60, v60, v62, v62
v_maximum3_f32 v46, v46, v48, v48
v_mov_b32 v62, v60
buffer_load_dwordx4 v0, s[4:7], s47 offen offset:2048 sc1 lds
v_mov_b32 v48, v46
v_permlane16_swap_b32 v60, v62
s_nop 0
v_permlane16_swap_b32 v46, v48
v_maximum3_f32 v60, v60, v62, v62
v_maximum3_f32 v46, v46, v48, v48
v_mul_f32_e64 v60, -v60, s16
v_pk_fma_f32 v[228:229], v[228:229], s[16:17], v[60:61] op_sel_hi:[1,0,0]
v_pk_fma_f32 v[230:231], v[230:231], s[16:17], v[60:61] op_sel_hi:[1,0,0]
v_pk_fma_f32 v[232:233], v[232:233], s[16:17], v[60:61] op_sel_hi:[1,0,0]
v_pk_fma_f32 v[234:235], v[234:235], s[16:17], v[60:61] op_sel_hi:[1,0,0]
v_exp_f32 v228, v228
v_exp_f32 v229, v229
v_exp_f32 v230, v230
buffer_load_dwordx4 v0, s[4:7], s47 offen offset:3072 sc1 lds
v_exp_f32 v231, v231
v_exp_f32 v232, v232
v_exp_f32 v233, v233
v_exp_f32 v234, v234
v_exp_f32 v235, v235
v_cvt_pk_bf16_f32 v228, v228, v229
v_cvt_pk_bf16_f32 v229, v230, v231
v_cvt_pk_bf16_f32 v230, v232, v233
v_cvt_pk_bf16_f32 v231, v234, v235
v_mul_f32_e64 v46, -v46, s16
v_pk_fma_f32 v[236:237], v[236:237], s[16:17], v[46:47] op_sel_hi:[1,0,0]
v_pk_fma_f32 v[238:239], v[238:239], s[16:17], v[46:47] op_sel_hi:[1,0,0]
s_add_u32 m0, m0, 0x1000
s_add_u32 s47, s47, 0x1000
buffer_load_dwordx4 v0, s[4:7], s47 offen sc1 lds
v_pk_fma_f32 v[240:241], v[240:241], s[16:17], v[46:47] op_sel_hi:[1,0,0]
v_pk_fma_f32 v[242:243], v[242:243], s[16:17], v[46:47] op_sel_hi:[1,0,0]
v_exp_f32 v236, v236
v_exp_f32 v237, v237
v_exp_f32 v238, v238
v_exp_f32 v239, v239
v_exp_f32 v240, v240
v_exp_f32 v241, v241
v_exp_f32 v242, v242
v_exp_f32 v243, v243
v_cvt_pk_bf16_f32 v236, v236, v237
v_cvt_pk_bf16_f32 v237, v238, v239
v_cvt_pk_bf16_f32 v238, v240, v241
v_cvt_pk_bf16_f32 v239, v242, v243
v_mfma_f32_16x16x32_bf16 v[56:59], v[222:225], v[228:231], 0
v_mfma_f32_16x16x32_bf16 v[52:55], v[212:215], v[228:231], 0
v_mfma_f32_16x16x32_bf16 v[248:251], v[224:227], v[236:239], 0
v_mfma_f32_16x16x32_bf16 v[244:247], v[214:217], v[236:239], 0
v_cmp_gt_u32_e32 vcc, 0x200, v0
s_mul_i32 s44, s36, 0xa00
s_add_u32 s44, s44, s64
s_add_u32 s78, s44, 0x810000
v_cndmask_b32 v64, v9, v0, vcc
v_rcp_f32 v58, v57
s_nop 0
v_rcp_f32 v250, v249
v_pk_mul_f32 v[52:53], v[52:53], v[58:59] op_sel_hi:[1,0]
v_pk_mul_f32 v[54:55], v[54:55], v[58:59] op_sel_hi:[1,0]
v_mul_f32 v56, v56, v58
v_cvt_pk_bf16_f32 v52, v52, v53
v_cvt_pk_bf16_f32 v53, v54, v55
v_cvt_pk_bf16_f32 v56, v56, v56
ds_write_b64 v42, v[52:53] offset:5632
ds_write_b16 v43, v56 offset:5664
v_pk_mul_f32 v[244:245], v[244:245], v[250:251] op_sel_hi:[1,0]
v_pk_mul_f32 v[246:247], v[246:247], v[250:251] op_sel_hi:[1,0]
v_mul_f32 v248, v248, v250
v_cvt_pk_bf16_f32 v244, v244, v245
v_cvt_pk_bf16_f32 v245, v246, v247
v_cvt_pk_bf16_f32 v248, v248, v248
ds_write_b64 v42, v[244:245] offset:8448
ds_write_b16 v43, v248 offset:8480
s_waitcnt lgkmcnt(0)
s_barrier
ds_read_b128 v[68:71], v45
ds_read_b128 v[72:75], v45 offset:64
ds_read_b128 v[76:79], v45 offset:128
s_waitcnt lgkmcnt(2)
buffer_store_dwordx4 v[68:71], v0, s[4:7], s78 offen sc1
s_waitcnt lgkmcnt(1)
buffer_store_dwordx4 v[72:75], v0, s[4:7], s78 offen offset:1024 sc1
s_waitcnt lgkmcnt(0)
buffer_store_dwordx4 v[76:79], v64, s[4:7], s78 offen offset:2048 sc1
s_add_u32 s47, s60, 0xf000
s_add_u32 m0, s61, 0xf000
s_nop 0
buffer_load_dwordx4 v0, s[4:7], s47 offen sc1 lds
buffer_load_dwordx4 v0, s[4:7], s47 offen offset:1024 sc1 lds
buffer_load_dwordx4 v0, s[4:7], s47 offen offset:2048 sc1 lds
buffer_load_dwordx4 v0, s[4:7], s47 offen offset:3072 sc1 lds
s_add_u32 m0, m0, 0x1000
s_add_u32 s47, s47, 0x1000
buffer_load_dwordx4 v0, s[4:7], s47 offen sc1 lds
s_mul_i32 s46, s34, 0x1400
s_mul_i32 s45, s35, 0x14000
s_add_u32 s46, s46, s45
s_add_u32 s46, s46, 0x810000
s_add_u32 s45, s34, 1
s_and_b32 s45, s45, 3
s_mul_i32 s45, s45, 0x5000
s_add_u32 s80, s45, s46
s_add_u32 s45, s34, 2
s_and_b32 s45, s45, 3
s_mul_i32 s45, s45, 0x5000
s_add_u32 s81, s45, s46
s_add_u32 s45, s34, 3
s_and_b32 s45, s45, 3
s_mul_i32 s45, s45, 0x5000
s_add_u32 s82, s45, s46
s_add_u32 s45, s34, 4
s_and_b32 s45, s45, 3
s_mul_i32 s45, s45, 0x5000
s_add_u32 s77, s45, s46
s_add_u32 s45, s34, 1
s_and_b32 s45, s45, 3
s_mul_i32 s45, s45, 0x3c00
s_add_u32 s45, s45, 0
v_add_u32 v20, s45, v0
s_add_u32 s45, s34, 2
s_and_b32 s45, s45, 3
s_mul_i32 s45, s45, 0x3c00
s_add_u32 s45, s45, 0x1400
v_add_u32 v21, s45, v0
s_add_u32 s45, s34, 3
s_and_b32 s45, s45, 3
s_mul_i32 s45, s45, 0x3c00
s_add_u32 s45, s45, 0x2800
v_add_u32 v22, s45, v0
s_mul_i32 s45, s34, 0x3c00
v_add_u32 v23, s45, v0
v_lshl_add_u32 v24, s65, 10, v0
v_lshl_add_u32 v25, s66, 10, v0
v_lshl_add_u32 v26, s67, 10, v0
v_lshl_add_u32 v27, s68, 10, v0
v_lshl_add_u32 v28, s69, 10, v0
v_lshlrev_b32 v172, 16, v10
v_and_b32 v173, s59, v10
v_lshlrev_b32 v174, 16, v11
v_and_b32 v175, s59, v11
v_lshlrev_b32 v176, 16, v12
v_and_b32 v177, s59, v12
v_lshlrev_b32 v178, 16, v13
v_and_b32 v179, s59, v13
v_lshlrev_b32 v180, 16, v14
v_and_b32 v181, s59, v14
)ASM"
R"ASM(
v_lshlrev_b32 v182, 16, v15
v_and_b32 v183, s59, v15
v_lshlrev_b32 v184, 16, v16
v_and_b32 v185, s59, v16
v_lshlrev_b32 v186, 16, v17
v_and_b32 v187, s59, v17
v_lshlrev_b32 v188, 16, v18
v_and_b32 v189, s59, v18
v_lshlrev_b32 v190, 16, v19
v_and_b32 v191, s59, v19
s_cmp_eq_u32 s34, 3
s_cselect_b32 s83, 0x80000000, 0
s_lshl_b32 s43, s35, 7
s_add_u32 s43, s43, 0x2000
s_lshl_b32 s44, s36, 3
s_add_u32 s44, s44, s43
v_and_b32 v30, 56, v1
s_waitcnt vmcnt(5)
s_barrier
buffer_store_dwordx2 v[4:5], v2, s[4:7], s44 offen sc1
s_add_u32 s47, s60, 0x14000
s_add_u32 m0, s61, 0x14000
s_nop 0
buffer_load_dwordx4 v0, s[4:7], s47 offen sc1 lds
buffer_load_dwordx4 v0, s[4:7], s47 offen offset:1024 sc1 lds
buffer_load_dwordx4 v0, s[4:7], s47 offen offset:2048 sc1 lds
buffer_load_dwordx4 v0, s[4:7], s47 offen offset:3072 sc1 lds
s_add_u32 m0, m0, 0x1000
s_add_u32 s47, s47, 0x1000
buffer_load_dwordx4 v0, s[4:7], s47 offen sc1 lds
s_add_u32 s47, s60, 0xa000
s_add_u32 m0, s61, 0xa000
s_nop 0
buffer_load_dwordx4 v0, s[4:7], s47 offen sc1 lds
buffer_load_dwordx4 v0, s[4:7], s47 offen offset:1024 sc1 lds
buffer_load_dwordx4 v0, s[4:7], s47 offen offset:2048 sc1 lds
buffer_load_dwordx4 v0, s[4:7], s47 offen offset:3072 sc1 lds
s_add_u32 m0, m0, 0x1000
s_add_u32 s47, s47, 0x1000
buffer_load_dwordx4 v0, s[4:7], s47 offen sc1 lds
s_cmp_lg_u32 s34, 0
s_cbranch_scc1 .Lk_join_7
s_mov_b32 s42, 0x10000
.Lk_fol_8:
buffer_load_dwordx2 v[226:227], v30, s[4:7], s43 offen sc1
s_sub_u32 s42, s42, 1
s_cselect_b64 s[48:49], -1, 0
s_waitcnt vmcnt(0)
v_cmp_eq_u64_e32 vcc, s[40:41], v[226:227]
s_nop 0
s_or_b64 vcc, vcc, s[48:49]
s_cmp_eq_u64 vcc, exec
s_cbranch_scc0 .Lk_fol_8
s_nop 0
.Lk_join_7:
s_barrier
buffer_load_dwordx4 v[32:35], v24, s[4:7], s80 offen sc1
buffer_load_dwordx4 v[36:39], v24, s[4:7], s81 offen sc1
buffer_load_dwordx4 v[40:43], v24, s[4:7], s82 offen sc1
buffer_load_dwordx4 v[44:47], v24, s[4:7], s77 offen sc1
buffer_load_dwordx4 v[48:51], v25, s[4:7], s80 offen sc1
buffer_load_dwordx4 v[52:55], v25, s[4:7], s81 offen sc1
buffer_load_dwordx4 v[56:59], v25, s[4:7], s82 offen sc1
buffer_load_dwordx4 v[60:63], v25, s[4:7], s77 offen sc1
s_waitcnt vmcnt(7)
v_mfma_f32_16x16x32_bf16 v[112:115], a[156:159], v[32:35], 0
buffer_load_dwordx4 v[64:67], v26, s[4:7], s80 offen sc1
v_mfma_f32_16x16x32_bf16 v[116:119], a[160:163], v[32:35], 0
buffer_load_dwordx4 v[68:71], v26, s[4:7], s81 offen sc1
v_mfma_f32_16x16x32_bf16 v[120:123], a[164:167], v[32:35], 0
buffer_load_dwordx4 v[72:75], v26, s[4:7], s82 offen sc1
v_mfma_f32_16x16x32_bf16 v[124:127], a[168:171], v[32:35], 0
buffer_load_dwordx4 v[76:79], v26, s[4:7], s77 offen sc1
v_mfma_f32_16x16x32_bf16 v[128:131], a[172:175], v[32:35], 0
buffer_load_dwordx4 v[80:83], v27, s[4:7], s80 offen sc1
s_waitcnt vmcnt(11)
v_mfma_f32_16x16x32_bf16 v[132:135], a[156:159], v[36:39], 0
buffer_load_dwordx4 v[84:87], v27, s[4:7], s81 offen sc1
v_mfma_f32_16x16x32_bf16 v[136:139], a[160:163], v[36:39], 0
buffer_load_dwordx4 v[88:91], v27, s[4:7], s82 offen sc1
v_mfma_f32_16x16x32_bf16 v[140:143], a[164:167], v[36:39], 0
buffer_load_dwordx4 v[92:95], v27, s[4:7], s77 offen sc1
v_mfma_f32_16x16x32_bf16 v[144:147], a[168:171], v[36:39], 0
v_mfma_f32_16x16x32_bf16 v[148:151], a[172:175], v[36:39], 0
s_waitcnt vmcnt(13)
v_mfma_f32_16x16x32_bf16 v[152:155], a[156:159], v[40:43], 0
v_mfma_f32_16x16x32_bf16 v[156:159], a[160:163], v[40:43], 0
v_mfma_f32_16x16x32_bf16 v[160:163], a[164:167], v[40:43], 0
v_mfma_f32_16x16x32_bf16 v[164:167], a[168:171], v[40:43], 0
v_mfma_f32_16x16x32_bf16 v[168:171], a[172:175], v[40:43], 0
s_waitcnt vmcnt(12)
v_mfma_f32_16x16x32_bf16 v[172:175], a[156:159], v[44:47], v[172:175]
v_mfma_f32_16x16x32_bf16 v[176:179], a[160:163], v[44:47], v[176:179]
v_mfma_f32_16x16x32_bf16 v[180:183], a[164:167], v[44:47], v[180:183]
v_mfma_f32_16x16x32_bf16 v[184:187], a[168:171], v[44:47], v[184:187]
v_mfma_f32_16x16x32_bf16 v[188:191], a[172:175], v[44:47], v[188:191]
s_waitcnt vmcnt(11)
v_mfma_f32_16x16x32_bf16 v[112:115], a[176:179], v[48:51], v[112:115]
buffer_load_dwordx4 v[96:99], v28, s[4:7], s80 offen sc1
v_mfma_f32_16x16x32_bf16 v[116:119], a[180:183], v[48:51], v[116:119]
v_mfma_f32_16x16x32_bf16 v[120:123], a[184:187], v[48:51], v[120:123]
buffer_load_dwordx4 v[100:103], v28, s[4:7], s81 offen sc1
v_mfma_f32_16x16x32_bf16 v[124:127], a[188:191], v[48:51], v[124:127]
v_mfma_f32_16x16x32_bf16 v[128:131], a[192:195], v[48:51], v[128:131]
buffer_load_dwordx4 v[104:107], v28, s[4:7], s82 offen sc1
s_waitcnt vmcnt(13)
v_mfma_f32_16x16x32_bf16 v[132:135], a[176:179], v[52:55], v[132:135]
v_mfma_f32_16x16x32_bf16 v[136:139], a[180:183], v[52:55], v[136:139]
buffer_load_dwordx4 v[108:111], v28, s[4:7], s77 offen sc1
v_mfma_f32_16x16x32_bf16 v[140:143], a[184:187], v[52:55], v[140:143]
v_mfma_f32_16x16x32_bf16 v[144:147], a[188:191], v[52:55], v[144:147]
v_mfma_f32_16x16x32_bf16 v[148:151], a[192:195], v[52:55], v[148:151]
s_waitcnt vmcnt(13)
v_mfma_f32_16x16x32_bf16 v[152:155], a[176:179], v[56:59], v[152:155]
v_mfma_f32_16x16x32_bf16 v[156:159], a[180:183], v[56:59], v[156:159]
v_mfma_f32_16x16x32_bf16 v[160:163], a[184:187], v[56:59], v[160:163]
v_mfma_f32_16x16x32_bf16 v[164:167], a[188:191], v[56:59], v[164:167]
v_mfma_f32_16x16x32_bf16 v[168:171], a[192:195], v[56:59], v[168:171]
s_waitcnt vmcnt(12)
v_mfma_f32_16x16x32_bf16 v[172:175], a[176:179], v[60:63], v[172:175]
v_mfma_f32_16x16x32_bf16 v[176:179], a[180:183], v[60:63], v[176:179]
v_mfma_f32_16x16x32_bf16 v[180:183], a[184:187], v[60:63], v[180:183]
v_mfma_f32_16x16x32_bf16 v[184:187], a[188:191], v[60:63], v[184:187]
v_mfma_f32_16x16x32_bf16 v[188:191], a[192:195], v[60:63], v[188:191]
s_waitcnt vmcnt(11)
v_mfma_f32_16x16x32_bf16 v[112:115], a[196:199], v[64:67], v[112:115]
v_mfma_f32_16x16x32_bf16 v[116:119], a[200:203], v[64:67], v[116:119]
v_mfma_f32_16x16x32_bf16 v[120:123], a[204:207], v[64:67], v[120:123]
v_mfma_f32_16x16x32_bf16 v[124:127], a[208:211], v[64:67], v[124:127]
v_mfma_f32_16x16x32_bf16 v[128:131], a[212:215], v[64:67], v[128:131]
s_waitcnt vmcnt(10)
v_mfma_f32_16x16x32_bf16 v[132:135], a[196:199], v[68:71], v[132:135]
v_mfma_f32_16x16x32_bf16 v[136:139], a[200:203], v[68:71], v[136:139]
v_mfma_f32_16x16x32_bf16 v[140:143], a[204:207], v[68:71], v[140:143]
v_mfma_f32_16x16x32_bf16 v[144:147], a[208:211], v[68:71], v[144:147]
v_mfma_f32_16x16x32_bf16 v[148:151], a[212:215], v[68:71], v[148:151]
s_waitcnt vmcnt(9)
v_mfma_f32_16x16x32_bf16 v[152:155], a[196:199], v[72:75], v[152:155]
v_mfma_f32_16x16x32_bf16 v[156:159], a[200:203], v[72:75], v[156:159]
v_mfma_f32_16x16x32_bf16 v[160:163], a[204:207], v[72:75], v[160:163]
v_mfma_f32_16x16x32_bf16 v[164:167], a[208:211], v[72:75], v[164:167]
v_mfma_f32_16x16x32_bf16 v[168:171], a[212:215], v[72:75], v[168:171]
s_waitcnt vmcnt(8)
v_mfma_f32_16x16x32_bf16 v[172:175], a[196:199], v[76:79], v[172:175]
v_mfma_f32_16x16x32_bf16 v[176:179], a[200:203], v[76:79], v[176:179]
v_mfma_f32_16x16x32_bf16 v[180:183], a[204:207], v[76:79], v[180:183]
v_mfma_f32_16x16x32_bf16 v[184:187], a[208:211], v[76:79], v[184:187]
v_mfma_f32_16x16x32_bf16 v[188:191], a[212:215], v[76:79], v[188:191]
s_waitcnt vmcnt(7)
v_mfma_f32_16x16x32_bf16 v[112:115], a[216:219], v[80:83], v[112:115]
s_add_u32 s79, s96, 0
s_and_b32 s79, s79, 7
s_mul_i32 s79, s79, 0x1400
s_add_u32 s79, s79, s63
buffer_load_dwordx4 a[0:3], v0, s[4:7], s79 offen sc1
v_mfma_f32_16x16x32_bf16 v[116:119], a[220:223], v[80:83], v[116:119]
v_mfma_f32_16x16x32_bf16 v[120:123], a[224:227], v[80:83], v[120:123]
v_mfma_f32_16x16x32_bf16 v[124:127], a[228:231], v[80:83], v[124:127]
v_mfma_f32_16x16x32_bf16 v[128:131], a[232:235], v[80:83], v[128:131]
buffer_load_dwordx4 a[4:7], v0, s[4:7], s79 offen offset:1024 sc1
s_waitcnt vmcnt(5)
v_mfma_f32_16x16x32_bf16 v[112:115], a[236:239], v[96:99], v[112:115]
v_mfma_f32_16x16x32_bf16 v[116:119], a[240:243], v[96:99], v[116:119]
v_mfma_f32_16x16x32_bf16 v[120:123], a[244:247], v[96:99], v[120:123]
buffer_load_dwordx4 a[8:11], v0, s[4:7], s79 offen offset:2048 sc1
v_mfma_f32_16x16x32_bf16 v[124:127], a[248:251], v[96:99], v[124:127]
v_mfma_f32_16x16x32_bf16 v[128:131], a[252:255], v[96:99], v[128:131]
v_mfma_f32_16x16x32_bf16 v[132:135], a[216:219], v[84:87], v[132:135]
buffer_load_dwordx4 a[12:15], v0, s[4:7], s79 offen offset:3072 sc1
v_mfma_f32_16x16x32_bf16 v[136:139], a[220:223], v[84:87], v[136:139]
v_mfma_f32_16x16x32_bf16 v[140:143], a[224:227], v[84:87], v[140:143]
ds_write_b128 v20, v[112:115]
s_add_u32 s79, s79, 0x1000
buffer_load_dwordx4 a[16:19], v0, s[4:7], s79 offen sc1
v_mfma_f32_16x16x32_bf16 v[144:147], a[228:231], v[84:87], v[144:147]
ds_write_b128 v20, v[116:119] offset:1024
v_mfma_f32_16x16x32_bf16 v[148:151], a[232:235], v[84:87], v[148:151]
s_add_u32 s79, s96, 1
s_and_b32 s79, s79, 7
s_mul_i32 s79, s79, 0x1400
s_add_u32 s79, s79, s63
buffer_load_dwordx4 a[20:23], v0, s[4:7], s79 offen sc1
ds_write_b128 v20, v[120:123] offset:2048
s_waitcnt vmcnt(8)
v_mfma_f32_16x16x32_bf16 v[132:135], a[236:239], v[100:103], v[132:135]
ds_write_b128 v20, v[124:127] offset:3072
buffer_load_dwordx4 a[24:27], v0, s[4:7], s79 offen offset:1024 sc1
v_mfma_f32_16x16x32_bf16 v[136:139], a[240:243], v[100:103], v[136:139]
ds_write_b128 v20, v[128:131] offset:4096
v_mfma_f32_16x16x32_bf16 v[140:143], a[244:247], v[100:103], v[140:143]
buffer_load_dwordx4 a[28:31], v0, s[4:7], s79 offen offset:2048 sc1
v_mfma_f32_16x16x32_bf16 v[144:147], a[248:251], v[100:103], v[144:147]
v_mfma_f32_16x16x32_bf16 v[148:151], a[252:255], v[100:103], v[148:151]
v_mfma_f32_16x16x32_bf16 v[152:155], a[216:219], v[88:91], v[152:155]
buffer_load_dwordx4 a[32:35], v0, s[4:7], s79 offen offset:3072 sc1
v_mfma_f32_16x16x32_bf16 v[156:159], a[220:223], v[88:91], v[156:159]
v_mfma_f32_16x16x32_bf16 v[160:163], a[224:227], v[88:91], v[160:163]
ds_write_b128 v21, v[132:135]
s_add_u32 s79, s79, 0x1000
buffer_load_dwordx4 a[36:39], v0, s[4:7], s79 offen sc1
)ASM"
R"ASM(
v_mfma_f32_16x16x32_bf16 v[164:167], a[228:231], v[88:91], v[164:167]
ds_write_b128 v21, v[136:139] offset:1024
v_mfma_f32_16x16x32_bf16 v[168:171], a[232:235], v[88:91], v[168:171]
s_add_u32 s79, s96, 2
s_and_b32 s79, s79, 7
s_mul_i32 s79, s79, 0x1400
s_add_u32 s79, s79, s63
buffer_load_dwordx4 a[40:43], v0, s[4:7], s79 offen sc1
ds_write_b128 v21, v[140:143] offset:2048
s_waitcnt vmcnt(12)
v_mfma_f32_16x16x32_bf16 v[152:155], a[236:239], v[104:107], v[152:155]
ds_write_b128 v21, v[144:147] offset:3072
buffer_load_dwordx4 a[44:47], v0, s[4:7], s79 offen offset:1024 sc1
v_mfma_f32_16x16x32_bf16 v[156:159], a[240:243], v[104:107], v[156:159]
ds_write_b128 v21, v[148:151] offset:4096
v_mfma_f32_16x16x32_bf16 v[160:163], a[244:247], v[104:107], v[160:163]
buffer_load_dwordx4 a[48:51], v0, s[4:7], s79 offen offset:2048 sc1
v_mfma_f32_16x16x32_bf16 v[164:167], a[248:251], v[104:107], v[164:167]
v_mfma_f32_16x16x32_bf16 v[168:171], a[252:255], v[104:107], v[168:171]
v_mfma_f32_16x16x32_bf16 v[172:175], a[216:219], v[92:95], v[172:175]
buffer_load_dwordx4 a[52:55], v0, s[4:7], s79 offen offset:3072 sc1
v_mfma_f32_16x16x32_bf16 v[176:179], a[220:223], v[92:95], v[176:179]
v_mfma_f32_16x16x32_bf16 v[180:183], a[224:227], v[92:95], v[180:183]
ds_write_b128 v22, v[152:155]
s_add_u32 s79, s79, 0x1000
buffer_load_dwordx4 a[56:59], v0, s[4:7], s79 offen sc1
v_mfma_f32_16x16x32_bf16 v[184:187], a[228:231], v[92:95], v[184:187]
ds_write_b128 v22, v[156:159] offset:1024
v_mfma_f32_16x16x32_bf16 v[188:191], a[232:235], v[92:95], v[188:191]
s_add_u32 s79, s96, 3
s_and_b32 s79, s79, 7
s_mul_i32 s79, s79, 0x1400
s_add_u32 s79, s79, s63
buffer_load_dwordx4 a[60:63], v0, s[4:7], s79 offen sc1
ds_write_b128 v22, v[160:163] offset:2048
s_waitcnt vmcnt(16)
v_mfma_f32_16x16x32_bf16 v[172:175], a[236:239], v[108:111], v[172:175]
ds_write_b128 v22, v[164:167] offset:3072
v_mfma_f32_16x16x32_bf16 v[176:179], a[240:243], v[108:111], v[176:179]
ds_write_b128 v22, v[168:171] offset:4096
v_mfma_f32_16x16x32_bf16 v[180:183], a[244:247], v[108:111], v[180:183]
v_mfma_f32_16x16x32_bf16 v[184:187], a[248:251], v[108:111], v[184:187]
v_mfma_f32_16x16x32_bf16 v[188:191], a[252:255], v[108:111], v[188:191]
s_lshr_b32 s43, s36, 1
s_mul_i32 s43, s43, 0x1400
s_add_u32 s43, s43, s64
s_add_u32 s43, s43, 0xa90000
s_and_b32 s44, s36, 1
s_lshl_b32 s45, s44, 11
s_add_u32 s45, s45, s43
s_mov_b32 s78, s45
s_waitcnt lgkmcnt(0)
s_barrier
ds_read_b128 v[32:35], v23
ds_read_b128 v[52:55], v23 offset:5120
ds_read_b128 v[72:75], v23 offset:10240
ds_read_b128 v[36:39], v23 offset:1024
ds_read_b128 v[56:59], v23 offset:6144
ds_read_b128 v[76:79], v23 offset:11264
ds_read_b128 v[40:43], v23 offset:2048
ds_read_b128 v[60:63], v23 offset:7168
ds_read_b128 v[80:83], v23 offset:12288
ds_read_b128 v[44:47], v23 offset:3072
ds_read_b128 v[64:67], v23 offset:8192
ds_read_b128 v[84:87], v23 offset:13312
ds_read_b128 v[48:51], v23 offset:4096
ds_read_b128 v[68:71], v23 offset:9216
ds_read_b128 v[88:91], v23 offset:14336
s_lshl_b32 s44, s44, 9
s_add_u32 s44, s44, s43
s_add_u32 s44, s44, 0x1000
s_lshl_b32 s43, s35, 2
s_add_u32 s43, s43, s34
s_lshl_b32 s43, s43, 10
s_lshl_b32 s46, s36, 7
s_add_u32 s43, s43, s46
s_add_u32 s43, s43, 0xd10000
v_cmp_gt_u32_e32 vcc, 0x80, v1
s_waitcnt lgkmcnt(13)
v_pk_add_f32 v[32:33], v[32:33], v[52:53]
v_pk_add_f32 v[34:35], v[34:35], v[54:55]
s_waitcnt lgkmcnt(12)
v_pk_add_f32 v[172:173], v[172:173], v[72:73]
v_pk_add_f32 v[174:175], v[174:175], v[74:75]
v_pk_add_f32 v[172:173], v[172:173], v[32:33]
v_pk_add_f32 v[174:175], v[174:175], v[34:35]
v_cvt_pk_bf16_f32 v10, v172, v173
v_cvt_pk_bf16_f32 v11, v174, v175
v_pk_add_f32 v[96:97], v[172:173], v[174:175]
v_pk_mul_f32 v[98:99], v[172:173], v[172:173]
v_cndmask_b32 v102, v9, v1, vcc
v_pk_fma_f32 v[98:99], v[174:175], v[174:175], v[98:99] op_sel_hi:[1,1,1]
s_waitcnt lgkmcnt(10)
v_pk_add_f32 v[36:37], v[36:37], v[56:57]
v_pk_add_f32 v[38:39], v[38:39], v[58:59]
s_waitcnt lgkmcnt(9)
v_pk_add_f32 v[176:177], v[176:177], v[76:77]
v_pk_add_f32 v[178:179], v[178:179], v[78:79]
v_pk_add_f32 v[176:177], v[176:177], v[36:37]
v_pk_add_f32 v[178:179], v[178:179], v[38:39]
v_cvt_pk_bf16_f32 v12, v176, v177
v_cvt_pk_bf16_f32 v13, v178, v179
v_pk_add_f32 v[96:97], v[96:97], v[176:177]
v_pk_add_f32 v[96:97], v[96:97], v[178:179]
v_pk_fma_f32 v[98:99], v[176:177], v[176:177], v[98:99] op_sel_hi:[1,1,1]
v_pk_fma_f32 v[98:99], v[178:179], v[178:179], v[98:99] op_sel_hi:[1,1,1]
buffer_store_dwordx4 v[10:13], v0, s[4:7], s45 offen sc1
s_waitcnt lgkmcnt(7)
v_pk_add_f32 v[40:41], v[40:41], v[60:61]
v_pk_add_f32 v[42:43], v[42:43], v[62:63]
s_waitcnt lgkmcnt(6)
v_pk_add_f32 v[180:181], v[180:181], v[80:81]
v_pk_add_f32 v[182:183], v[182:183], v[82:83]
v_pk_add_f32 v[180:181], v[180:181], v[40:41]
v_pk_add_f32 v[182:183], v[182:183], v[42:43]
v_cvt_pk_bf16_f32 v14, v180, v181
v_cvt_pk_bf16_f32 v15, v182, v183
v_pk_add_f32 v[96:97], v[96:97], v[180:181]
v_pk_add_f32 v[96:97], v[96:97], v[182:183]
v_pk_fma_f32 v[98:99], v[180:181], v[180:181], v[98:99] op_sel_hi:[1,1,1]
v_pk_fma_f32 v[98:99], v[182:183], v[182:183], v[98:99] op_sel_hi:[1,1,1]
s_waitcnt lgkmcnt(4)
v_pk_add_f32 v[44:45], v[44:45], v[64:65]
v_pk_add_f32 v[46:47], v[46:47], v[66:67]
s_waitcnt lgkmcnt(3)
v_pk_add_f32 v[184:185], v[184:185], v[84:85]
v_pk_add_f32 v[186:187], v[186:187], v[86:87]
v_pk_add_f32 v[184:185], v[184:185], v[44:45]
v_pk_add_f32 v[186:187], v[186:187], v[46:47]
v_cvt_pk_bf16_f32 v16, v184, v185
v_cvt_pk_bf16_f32 v17, v186, v187
v_pk_add_f32 v[96:97], v[96:97], v[184:185]
v_pk_add_f32 v[96:97], v[96:97], v[186:187]
v_pk_fma_f32 v[98:99], v[184:185], v[184:185], v[98:99] op_sel_hi:[1,1,1]
v_pk_fma_f32 v[98:99], v[186:187], v[186:187], v[98:99] op_sel_hi:[1,1,1]
buffer_store_dwordx4 v[14:17], v0, s[4:7], s45 offen offset:1024 sc1
s_waitcnt lgkmcnt(1)
v_pk_add_f32 v[48:49], v[48:49], v[68:69]
v_pk_add_f32 v[50:51], v[50:51], v[70:71]
s_waitcnt lgkmcnt(0)
v_pk_add_f32 v[188:189], v[188:189], v[88:89]
v_pk_add_f32 v[190:191], v[190:191], v[90:91]
v_pk_add_f32 v[188:189], v[188:189], v[48:49]
v_pk_add_f32 v[190:191], v[190:191], v[50:51]
v_cvt_pk_bf16_f32 v18, v188, v189
v_cvt_pk_bf16_f32 v19, v190, v191
v_pk_add_f32 v[96:97], v[96:97], v[188:189]
v_pk_add_f32 v[96:97], v[96:97], v[190:191]
v_pk_fma_f32 v[98:99], v[188:189], v[188:189], v[98:99] op_sel_hi:[1,1,1]
v_pk_fma_f32 v[98:99], v[190:191], v[190:191], v[98:99] op_sel_hi:[1,1,1]
buffer_store_dwordx2 v[18:19], v1, s[4:7], s44 offen sc1
v_add_f32 v96, v96, v97
v_add_f32 v97, v98, v99
v_mov_b32 v100, v96
v_mov_b32 v101, v97
s_nop 0
v_permlane32_swap_b32 v96, v100
v_permlane32_swap_b32 v97, v101
v_add_f32 v96, v96, v100
v_add_f32 v97, v97, v101
v_mov_b32 v100, v96
v_mov_b32 v101, v97
s_nop 0
v_permlane16_swap_b32 v96, v100
v_permlane16_swap_b32 v97, v101
v_add_f32 v96, v96, v100
v_add_f32 v97, v97, v101
buffer_store_dwordx2 v[96:97], v102, s[4:7], s43 offen sc1
v_lshrrev_b32 v46, 7, v0
v_lshlrev_b32 v46, 9, v46
v_and_b32 v47, 0x70, v0
v_or_b32 v46, v46, v47
s_mul_i32 s45, s38, 0x2800
s_lshl_b32 s46, s34, 7
s_add_u32 s45, s45, s46
s_add_u32 s45, s45, 0x7e8000
v_cmp_gt_u32_e32 vcc, 0x200, v0
buffer_load_dwordx4 v[32:35], v46, s[4:7], s45 offen sc1
s_add_u32 s45, s45, 0x1000
buffer_load_dwordx4 v[36:39], v46, s[4:7], s45 offen sc1
s_add_u32 s45, s45, 0x1000
v_cndmask_b32 v46, v9, v46, vcc
buffer_load_dwordx4 v[40:43], v46, s[4:7], s45 offen sc1
s_mov_b32 s12, s24
s_and_b32 s13, s25, 0xffff
s_mov_b32 s14, 0xd80
s_mov_b32 s15, 0x20000
s_lshl_b32 s45, s51, 5
s_lshl_b32 s46, s34, 6
s_add_u32 s45, s45, s46
v_lshrrev_b32 v46, 1, v47
buffer_load_dwordx2 v[44:45], v46, s[12:15], s45 offen
s_lshl_b32 s45, s37, 3
s_lshl_b32 s46, s34, 1
s_add_u32 s85, s45, s46
s_mul_i32 s45, s85, 0x5000
s_add_u32 s80, s45, 0xa90000
s_add_u32 s81, s80, 0x5000
s_lshl_b32 s43, s37, 7
s_lshl_b32 s44, s38, 3
)ASM"
R"ASM(
s_add_u32 s84, s43, s44
s_add_u32 s44, s84, 0x3000
s_add_u32 s43, s43, 0x3000
v_and_b32 v30, 0x78, v1
s_waitcnt vmcnt(4)
s_barrier
buffer_store_dwordx2 v[4:5], v2, s[4:7], s44 offen sc1
s_add_u32 s47, s60, 0x5000
s_add_u32 m0, s61, 0x5000
s_nop 0
buffer_load_dwordx4 v0, s[4:7], s47 offen sc1 lds
buffer_load_dwordx4 v0, s[4:7], s47 offen offset:1024 sc1 lds
buffer_load_dwordx4 v0, s[4:7], s47 offen offset:2048 sc1 lds
buffer_load_dwordx4 v0, s[4:7], s47 offen offset:3072 sc1 lds
s_add_u32 m0, m0, 0x1000
s_add_u32 s47, s47, 0x1000
buffer_load_dwordx4 v0, s[4:7], s47 offen sc1 lds
s_add_u32 s47, s60, 0
s_add_u32 m0, s61, 0
s_nop 0
buffer_load_dwordx4 v0, s[4:7], s47 offen sc1 lds
buffer_load_dwordx4 v0, s[4:7], s47 offen offset:1024 sc1 lds
buffer_load_dwordx4 v0, s[4:7], s47 offen offset:2048 sc1 lds
buffer_load_dwordx4 v0, s[4:7], s47 offen offset:3072 sc1 lds
s_add_u32 m0, m0, 0x1000
s_add_u32 s47, s47, 0x1000
buffer_load_dwordx4 v0, s[4:7], s47 offen sc1 lds
buffer_load_dwordx4 a[64:67], v0, s[4:7], s79 offen offset:1024 sc1
buffer_load_dwordx4 a[68:71], v0, s[4:7], s79 offen offset:2048 sc1
buffer_load_dwordx4 a[72:75], v0, s[4:7], s79 offen offset:3072 sc1
s_add_u32 s79, s79, 0x1000
buffer_load_dwordx4 a[76:79], v0, s[4:7], s79 offen sc1
s_add_u32 s79, s96, 4
s_and_b32 s79, s79, 7
s_mul_i32 s79, s79, 0x1400
s_add_u32 s79, s79, s63
buffer_load_dwordx4 a[80:83], v0, s[4:7], s79 offen sc1
buffer_load_dwordx4 a[84:87], v0, s[4:7], s79 offen offset:1024 sc1
s_waitcnt vmcnt(19)
v_pk_add_f32 v[32:33], v[32:33], v[36:37]
v_pk_add_f32 v[34:35], v[34:35], v[38:39]
s_waitcnt vmcnt(18)
v_pk_add_f32 v[32:33], v[32:33], v[40:41]
v_pk_add_f32 v[34:35], v[34:35], v[42:43]
s_nop 0
v_add_f32_dpp v32, v32, v32 row_ror:8 row_mask:0xf bank_mask:0xf
v_add_f32_dpp v33, v33, v33 row_ror:8 row_mask:0xf bank_mask:0xf
v_add_f32_dpp v34, v34, v34 row_ror:8 row_mask:0xf bank_mask:0xf
v_add_f32_dpp v35, v35, v35 row_ror:8 row_mask:0xf bank_mask:0xf
v_mov_b32 v48, v32
v_mov_b32 v49, v33
v_mov_b32 v50, v34
v_mov_b32 v51, v35
v_permlane32_swap_b32 v32, v48
v_permlane32_swap_b32 v33, v49
v_permlane32_swap_b32 v34, v50
v_permlane32_swap_b32 v35, v51
v_add_f32 v32, v32, v48
v_add_f32 v33, v33, v49
v_add_f32 v34, v34, v50
v_add_f32 v35, v35, v51
v_mov_b32 v48, v32
v_mov_b32 v49, v33
v_mov_b32 v50, v34
v_mov_b32 v51, v35
v_permlane16_swap_b32 v32, v48
v_permlane16_swap_b32 v33, v49
v_permlane16_swap_b32 v34, v50
v_permlane16_swap_b32 v35, v51
v_add_f32 v32, v32, v48
v_add_f32 v33, v33, v49
v_add_f32 v34, v34, v50
v_add_f32 v35, v35, v51
s_waitcnt vmcnt(17)
v_lshlrev_b32 v48, 16, v44
v_and_b32 v49, s59, v44
v_lshlrev_b32 v50, 16, v45
v_and_b32 v51, s59, v45
s_lshl_b32 s44, s34, 7
v_add_u32 v47, s44, v47
ds_write_b128 v47, v[32:35]
ds_write_b128 v47, v[48:51] offset:512
s_mul_i32 s45, s85, 0xe000
s_lshr_b32 s44, s38, 2
s_mul_i32 s44, s44, 0x3800
s_add_u32 s45, s45, s44
s_add_u32 s18, s45, 0xd30000
s_and_b32 s44, s38, 3
s_lshl_b32 s45, s44, 11
s_add_u32 s82, s18, s45
s_add_u32 s77, s82, 0xe000
s_mov_b32 s16, 0xbdd2d3e8
v_lshrrev_b32 v28, 8, v0
v_lshl_or_b32 v28, v28, 8, v30
s_lshl_b32 s88, s85, 10
s_add_u32 s88, s88, 0xd10000
v_add_u32 v22, 0x23000, v0
v_add_u32 v23, 0x14000, v0
v_add_u32 v20, s71, v0
ds_read_b128 v[222:225], v22
ds_read_b128 v[226:229], v23 offset:40960
ds_read_b128 v[230:233], v23 offset:20480
ds_read_b128 v[234:237], v23
s_cmp_lg_u32 s34, 0
s_cbranch_scc1 .Lk_join_10
s_mov_b32 s42, 0x10000
.Lk_fhl_11:
buffer_load_dwordx2 v[24:25], v30, s[4:7], s43 offen sc1
s_sub_u32 s42, s42, 1
s_cselect_b64 s[48:49], -1, 0
s_waitcnt vmcnt(0)
v_cmp_eq_u64_e32 vcc, s[40:41], v[24:25]
s_nop 0
s_or_b64 vcc, vcc, s[48:49]
s_cmp_eq_u64 vcc, exec
s_cbranch_scc0 .Lk_fhl_11
s_nop 0
.Lk_join_10:
s_waitcnt lgkmcnt(0)
s_barrier
buffer_load_dwordx4 v[30:33], v20, s[4:7], s80 offen sc1
buffer_load_dwordx4 v[34:37], v20, s[4:7], s81 offen sc1
buffer_load_dwordx4 v[38:41], v20, s[4:7], s80 offen offset:1024 sc1
buffer_load_dwordx4 v[42:45], v20, s[4:7], s81 offen offset:1024 sc1
buffer_load_dwordx4 v[46:49], v20, s[4:7], s80 offen offset:2048 sc1
buffer_load_dwordx4 v[50:53], v20, s[4:7], s81 offen offset:2048 sc1
buffer_load_dwordx4 v[54:57], v20, s[4:7], s80 offen offset:3072 sc1
buffer_load_dwordx4 v[58:61], v20, s[4:7], s81 offen offset:3072 sc1
s_waitcnt vmcnt(7)
v_mfma_f32_16x16x32_bf16 v[190:193], v[222:225], v[30:33], 0
ds_read_b128 v[238:241], v22 offset:1024
s_waitcnt vmcnt(6)
v_mfma_f32_16x16x32_bf16 v[206:209], v[222:225], v[34:37], 0
v_add_u32 v21, s71, v1
v_add_u32 v21, 0x1000, v21
buffer_load_dwordx2 v[62:63], v21, s[4:7], s80 offen sc1
buffer_load_dwordx2 v[64:65], v21, s[4:7], s80 offen offset:512 sc1
v_mfma_f32_16x16x32_bf16 v[194:197], v[226:229], v[30:33], 0
ds_read_b128 v[242:245], v23 offset:41984
v_mfma_f32_16x16x32_bf16 v[210:213], v[226:229], v[34:37], 0
buffer_load_dwordx2 v[182:183], v28, s[4:7], s88 offen sc1
v_mfma_f32_16x16x32_bf16 v[198:201], v[230:233], v[30:33], 0
ds_read_b128 v[246:249], v23 offset:21504
v_mfma_f32_16x16x32_bf16 v[214:217], v[230:233], v[34:37], 0
buffer_load_dwordx2 v[66:67], v21, s[4:7], s81 offen sc1
buffer_load_dwordx2 v[68:69], v21, s[4:7], s81 offen offset:512 sc1
v_mfma_f32_16x16x32_bf16 v[202:205], v[234:237], v[30:33], 0
ds_read_b128 v[250:253], v23 offset:1024
v_mfma_f32_16x16x32_bf16 v[218:221], v[234:237], v[34:37], 0
buffer_load_dwordx2 v[184:185], v28, s[4:7], s88 offen offset:128 sc1
s_waitcnt vmcnt(11) lgkmcnt(3)
v_mfma_f32_16x16x32_bf16 v[190:193], v[238:241], v[38:41], v[190:193]
ds_read_b128 v[222:225], v22 offset:2048
s_waitcnt vmcnt(10)
v_mfma_f32_16x16x32_bf16 v[206:209], v[238:241], v[42:45], v[206:209]
v_add_u32 v20, s72, v0
buffer_load_dwordx4 v[70:73], v20, s[4:7], s80 offen sc1
s_waitcnt lgkmcnt(3)
v_mfma_f32_16x16x32_bf16 v[194:197], v[242:245], v[38:41], v[194:197]
ds_read_b128 v[226:229], v23 offset:43008
v_mfma_f32_16x16x32_bf16 v[210:213], v[242:245], v[42:45], v[210:213]
buffer_load_dwordx2 v[186:187], v28, s[4:7], s88 offen offset:1024 sc1
s_waitcnt lgkmcnt(3)
v_mfma_f32_16x16x32_bf16 v[198:201], v[246:249], v[38:41], v[198:201]
ds_read_b128 v[230:233], v23 offset:22528
v_mfma_f32_16x16x32_bf16 v[214:217], v[246:249], v[42:45], v[214:217]
buffer_load_dwordx4 v[74:77], v20, s[4:7], s81 offen sc1
s_waitcnt lgkmcnt(3)
v_mfma_f32_16x16x32_bf16 v[202:205], v[250:253], v[38:41], v[202:205]
ds_read_b128 v[234:237], v23 offset:2048
v_mfma_f32_16x16x32_bf16 v[218:221], v[250:253], v[42:45], v[218:221]
buffer_load_dwordx2 v[188:189], v28, s[4:7], s88 offen offset:1152 sc1
s_waitcnt vmcnt(13) lgkmcnt(3)
v_mfma_f32_16x16x32_bf16 v[190:193], v[222:225], v[46:49], v[190:193]
ds_read_b128 v[238:241], v22 offset:3072
s_waitcnt vmcnt(12)
v_mfma_f32_16x16x32_bf16 v[206:209], v[222:225], v[50:53], v[206:209]
buffer_load_dwordx4 v[78:81], v20, s[4:7], s80 offen offset:1024 sc1
s_waitcnt lgkmcnt(3)
v_mfma_f32_16x16x32_bf16 v[194:197], v[226:229], v[46:49], v[194:197]
ds_read_b128 v[242:245], v23 offset:44032
v_mfma_f32_16x16x32_bf16 v[210:213], v[226:229], v[50:53], v[210:213]
s_waitcnt lgkmcnt(3)
v_mfma_f32_16x16x32_bf16 v[198:201], v[230:233], v[46:49], v[198:201]
ds_read_b128 v[246:249], v23 offset:23552
v_mfma_f32_16x16x32_bf16 v[214:217], v[230:233], v[50:53], v[214:217]
buffer_load_dwordx4 v[82:85], v20, s[4:7], s81 offen offset:1024 sc1
s_waitcnt lgkmcnt(3)
v_mfma_f32_16x16x32_bf16 v[202:205], v[234:237], v[46:49], v[202:205]
ds_read_b128 v[250:253], v23 offset:3072
v_mfma_f32_16x16x32_bf16 v[218:221], v[234:237], v[50:53], v[218:221]
s_waitcnt vmcnt(13) lgkmcnt(3)
v_mfma_f32_16x16x32_bf16 v[190:193], v[238:241], v[54:57], v[190:193]
ds_read_b128 v[222:225], v22 offset:4096
s_waitcnt vmcnt(12)
v_mfma_f32_16x16x32_bf16 v[206:209], v[238:241], v[58:61], v[206:209]
buffer_load_dwordx4 v[86:89], v20, s[4:7], s80 offen offset:2048 sc1
s_waitcnt lgkmcnt(3)
v_mfma_f32_16x16x32_bf16 v[194:197], v[242:245], v[54:57], v[194:197]
ds_read_b128 v[226:229], v23 offset:45056
)ASM"
R"ASM(
v_mfma_f32_16x16x32_bf16 v[210:213], v[242:245], v[58:61], v[210:213]
s_waitcnt lgkmcnt(3)
v_mfma_f32_16x16x32_bf16 v[198:201], v[246:249], v[54:57], v[198:201]
ds_read_b128 v[230:233], v23 offset:24576
v_mfma_f32_16x16x32_bf16 v[214:217], v[246:249], v[58:61], v[214:217]
buffer_load_dwordx4 v[90:93], v20, s[4:7], s81 offen offset:2048 sc1
s_waitcnt lgkmcnt(3)
v_mfma_f32_16x16x32_bf16 v[202:205], v[250:253], v[54:57], v[202:205]
ds_read_b128 v[234:237], v23 offset:4096
v_mfma_f32_16x16x32_bf16 v[218:221], v[250:253], v[58:61], v[218:221]
s_waitcnt vmcnt(12) lgkmcnt(3)
v_mfma_f32_16x16x32_bf16 v[190:193], v[222:225], v[62:65], v[190:193]
ds_read_b128 v[238:241], v22 offset:5120
s_waitcnt vmcnt(9)
v_mfma_f32_16x16x32_bf16 v[206:209], v[222:225], v[66:69], v[206:209]
buffer_load_dwordx4 v[94:97], v20, s[4:7], s80 offen offset:3072 sc1
s_waitcnt lgkmcnt(3)
v_mfma_f32_16x16x32_bf16 v[194:197], v[226:229], v[62:65], v[194:197]
ds_read_b128 v[242:245], v23 offset:46080
v_mfma_f32_16x16x32_bf16 v[210:213], v[226:229], v[66:69], v[210:213]
s_waitcnt lgkmcnt(3)
v_mfma_f32_16x16x32_bf16 v[198:201], v[230:233], v[62:65], v[198:201]
ds_read_b128 v[246:249], v23 offset:25600
v_mfma_f32_16x16x32_bf16 v[214:217], v[230:233], v[66:69], v[214:217]
buffer_load_dwordx4 v[98:101], v20, s[4:7], s81 offen offset:3072 sc1
s_waitcnt lgkmcnt(3)
v_mfma_f32_16x16x32_bf16 v[202:205], v[234:237], v[62:65], v[202:205]
ds_read_b128 v[250:253], v23 offset:5120
v_mfma_f32_16x16x32_bf16 v[218:221], v[234:237], v[66:69], v[218:221]
s_waitcnt vmcnt(9) lgkmcnt(3)
v_mfma_f32_16x16x32_bf16 v[190:193], v[238:241], v[70:73], v[190:193]
ds_read_b128 v[222:225], v22 offset:6144
s_waitcnt vmcnt(7)
v_mfma_f32_16x16x32_bf16 v[206:209], v[238:241], v[74:77], v[206:209]
v_add_u32 v21, s72, v1
v_add_u32 v21, 0x1000, v21
buffer_load_dwordx2 v[102:103], v21, s[4:7], s80 offen sc1
buffer_load_dwordx2 v[104:105], v21, s[4:7], s80 offen offset:512 sc1
s_waitcnt lgkmcnt(3)
v_mfma_f32_16x16x32_bf16 v[194:197], v[242:245], v[70:73], v[194:197]
ds_read_b128 v[226:229], v23 offset:47104
v_mfma_f32_16x16x32_bf16 v[210:213], v[242:245], v[74:77], v[210:213]
v_add_f32 v24, v182, v184
v_add_f32 v25, v183, v185
s_waitcnt lgkmcnt(3)
v_mfma_f32_16x16x32_bf16 v[198:201], v[246:249], v[70:73], v[198:201]
ds_read_b128 v[230:233], v23 offset:26624
v_mfma_f32_16x16x32_bf16 v[214:217], v[246:249], v[74:77], v[214:217]
buffer_load_dwordx2 v[106:107], v21, s[4:7], s81 offen sc1
buffer_load_dwordx2 v[108:109], v21, s[4:7], s81 offen offset:512 sc1
s_waitcnt lgkmcnt(3)
v_mfma_f32_16x16x32_bf16 v[202:205], v[250:253], v[70:73], v[202:205]
ds_read_b128 v[234:237], v23 offset:6144
v_mfma_f32_16x16x32_bf16 v[218:221], v[250:253], v[74:77], v[218:221]
s_waitcnt vmcnt(10)
v_add_f32 v26, v186, v188
v_add_f32 v27, v187, v189
s_waitcnt vmcnt(9) lgkmcnt(3)
v_mfma_f32_16x16x32_bf16 v[190:193], v[222:225], v[78:81], v[190:193]
ds_read_b128 v[238:241], v22 offset:7168
s_waitcnt vmcnt(8)
v_mfma_f32_16x16x32_bf16 v[206:209], v[222:225], v[82:85], v[206:209]
v_add_u32 v20, s73, v0
buffer_load_dwordx4 v[110:113], v20, s[4:7], s80 offen sc1
v_mov_b32 v182, v24
v_mov_b32 v183, v25
s_waitcnt lgkmcnt(3)
v_mfma_f32_16x16x32_bf16 v[194:197], v[226:229], v[78:81], v[194:197]
ds_read_b128 v[242:245], v23 offset:48128
v_mfma_f32_16x16x32_bf16 v[210:213], v[226:229], v[82:85], v[210:213]
v_mov_b32 v184, v26
v_mov_b32 v185, v27
v_permlane32_swap_b32 v24, v182
s_waitcnt lgkmcnt(3)
v_mfma_f32_16x16x32_bf16 v[198:201], v[230:233], v[78:81], v[198:201]
ds_read_b128 v[246:249], v23 offset:27648
v_mfma_f32_16x16x32_bf16 v[214:217], v[230:233], v[82:85], v[214:217]
buffer_load_dwordx4 v[114:117], v20, s[4:7], s81 offen sc1
v_permlane32_swap_b32 v25, v183
v_permlane32_swap_b32 v26, v184
s_waitcnt lgkmcnt(3)
v_mfma_f32_16x16x32_bf16 v[202:205], v[234:237], v[78:81], v[202:205]
ds_read_b128 v[250:253], v23 offset:7168
v_mfma_f32_16x16x32_bf16 v[218:221], v[234:237], v[82:85], v[218:221]
v_permlane32_swap_b32 v27, v185
v_add_f32 v24, v24, v182
v_add_f32 v25, v25, v183
s_waitcnt vmcnt(9) lgkmcnt(3)
v_mfma_f32_16x16x32_bf16 v[190:193], v[238:241], v[86:89], v[190:193]
ds_read_b128 v[222:225], v22 offset:8192
s_waitcnt vmcnt(8)
v_mfma_f32_16x16x32_bf16 v[206:209], v[238:241], v[90:93], v[206:209]
buffer_load_dwordx4 v[118:121], v20, s[4:7], s80 offen offset:1024 sc1
s_waitcnt lgkmcnt(3)
v_mfma_f32_16x16x32_bf16 v[194:197], v[242:245], v[86:89], v[194:197]
ds_read_b128 v[226:229], v23 offset:49152
v_mfma_f32_16x16x32_bf16 v[210:213], v[242:245], v[90:93], v[210:213]
v_add_f32 v26, v26, v184
v_add_f32 v27, v27, v185
s_waitcnt lgkmcnt(3)
v_mfma_f32_16x16x32_bf16 v[198:201], v[246:249], v[86:89], v[198:201]
ds_read_b128 v[230:233], v23 offset:28672
v_mfma_f32_16x16x32_bf16 v[214:217], v[246:249], v[90:93], v[214:217]
buffer_load_dwordx4 v[122:125], v20, s[4:7], s81 offen offset:1024 sc1
s_waitcnt lgkmcnt(3)
v_mfma_f32_16x16x32_bf16 v[202:205], v[250:253], v[86:89], v[202:205]
ds_read_b128 v[234:237], v23 offset:8192
v_mfma_f32_16x16x32_bf16 v[218:221], v[250:253], v[90:93], v[218:221]
v_mov_b32 v182, v24
v_mov_b32 v183, v25
s_waitcnt vmcnt(9) lgkmcnt(3)
v_mfma_f32_16x16x32_bf16 v[190:193], v[222:225], v[94:97], v[190:193]
ds_read_b128 v[238:241], v22 offset:9216
s_waitcnt vmcnt(8)
v_mfma_f32_16x16x32_bf16 v[206:209], v[222:225], v[98:101], v[206:209]
buffer_load_dwordx4 v[126:129], v20, s[4:7], s80 offen offset:2048 sc1
v_mov_b32 v184, v26
v_mov_b32 v185, v27
s_waitcnt lgkmcnt(3)
v_mfma_f32_16x16x32_bf16 v[194:197], v[226:229], v[94:97], v[194:197]
ds_read_b128 v[242:245], v23 offset:50176
v_mfma_f32_16x16x32_bf16 v[210:213], v[226:229], v[98:101], v[210:213]
v_permlane16_swap_b32 v24, v182
v_permlane16_swap_b32 v25, v183
v_permlane16_swap_b32 v26, v184
s_waitcnt lgkmcnt(3)
v_mfma_f32_16x16x32_bf16 v[198:201], v[230:233], v[94:97], v[198:201]
ds_read_b128 v[246:249], v23 offset:29696
v_mfma_f32_16x16x32_bf16 v[214:217], v[230:233], v[98:101], v[214:217]
buffer_load_dwordx4 v[130:133], v20, s[4:7], s81 offen offset:2048 sc1
v_permlane16_swap_b32 v27, v185
v_add_f32 v24, v24, v182
s_waitcnt lgkmcnt(3)
v_mfma_f32_16x16x32_bf16 v[202:205], v[234:237], v[94:97], v[202:205]
ds_read_b128 v[250:253], v23 offset:9216
v_mfma_f32_16x16x32_bf16 v[218:221], v[234:237], v[98:101], v[218:221]
v_add_f32 v25, v25, v183
v_add_f32 v26, v26, v184
v_add_f32 v27, v27, v185
s_waitcnt vmcnt(8) lgkmcnt(3)
v_mfma_f32_16x16x32_bf16 v[190:193], v[238:241], v[102:105], v[190:193]
ds_read_b128 v[222:225], v22 offset:10240
s_waitcnt vmcnt(6)
v_mfma_f32_16x16x32_bf16 v[206:209], v[238:241], v[106:109], v[206:209]
buffer_load_dwordx4 v[134:137], v20, s[4:7], s80 offen offset:3072 sc1
s_waitcnt lgkmcnt(3)
v_mfma_f32_16x16x32_bf16 v[194:197], v[242:245], v[102:105], v[194:197]
ds_read_b128 v[226:229], v23 offset:51200
v_mfma_f32_16x16x32_bf16 v[210:213], v[242:245], v[106:109], v[210:213]
v_mul_f32_e64 v24, -v24, s57
v_mul_f32 v25, s57, v25
s_waitcnt lgkmcnt(3)
v_mfma_f32_16x16x32_bf16 v[198:201], v[246:249], v[102:105], v[198:201]
ds_read_b128 v[230:233], v23 offset:30720
v_mfma_f32_16x16x32_bf16 v[214:217], v[246:249], v[106:109], v[214:217]
buffer_load_dwordx4 v[138:141], v20, s[4:7], s81 offen offset:3072 sc1
s_waitcnt lgkmcnt(3)
v_mfma_f32_16x16x32_bf16 v[202:205], v[250:253], v[102:105], v[202:205]
ds_read_b128 v[234:237], v23 offset:10240
v_mfma_f32_16x16x32_bf16 v[218:221], v[250:253], v[106:109], v[218:221]
v_fma_f32 v25, -v24, v24, v25
v_max_f32 v25, 0, v25
s_waitcnt vmcnt(7) lgkmcnt(3)
v_mfma_f32_16x16x32_bf16 v[190:193], v[222:225], v[110:113], v[190:193]
ds_read_b128 v[238:241], v22 offset:11264
s_waitcnt vmcnt(6)
v_mfma_f32_16x16x32_bf16 v[206:209], v[222:225], v[114:117], v[206:209]
v_add_u32 v21, s73, v1
v_add_u32 v21, 0x1000, v21
buffer_load_dwordx2 v[142:143], v21, s[4:7], s80 offen sc1
buffer_load_dwordx2 v[144:145], v21, s[4:7], s80 offen offset:512 sc1
v_add_f32 v25, s58, v25
v_rsq_f32 v25, v25
s_waitcnt lgkmcnt(3)
v_mfma_f32_16x16x32_bf16 v[194:197], v[226:229], v[110:113], v[194:197]
ds_read_b128 v[242:245], v23 offset:52224
v_mfma_f32_16x16x32_bf16 v[210:213], v[226:229], v[114:117], v[210:213]
v_mul_f32_e64 v26, -v26, s57
v_mul_f32 v27, s57, v27
v_fma_f32 v27, -v26, v26, v27
s_waitcnt lgkmcnt(3)
v_mfma_f32_16x16x32_bf16 v[198:201], v[230:233], v[110:113], v[198:201]
ds_read_b128 v[246:249], v23 offset:31744
v_mfma_f32_16x16x32_bf16 v[214:217], v[230:233], v[114:117], v[214:217]
buffer_load_dwordx2 v[146:147], v21, s[4:7], s81 offen sc1
buffer_load_dwordx2 v[148:149], v21, s[4:7], s81 offen offset:512 sc1
v_max_f32 v27, 0, v27
v_add_f32 v27, s58, v27
s_waitcnt lgkmcnt(3)
v_mfma_f32_16x16x32_bf16 v[202:205], v[234:237], v[110:113], v[202:205]
ds_read_b128 v[250:253], v23 offset:11264
v_mfma_f32_16x16x32_bf16 v[218:221], v[234:237], v[114:117], v[218:221]
v_rsq_f32 v27, v27
v_mov_b32 v28, 0xc0135761
s_barrier
s_waitcnt vmcnt(9) lgkmcnt(3)
v_mfma_f32_16x16x32_bf16 v[190:193], v[238:241], v[118:121], v[190:193]
ds_read_b128 v[222:225], v22 offset:12288
s_waitcnt vmcnt(8)
v_mfma_f32_16x16x32_bf16 v[206:209], v[238:241], v[122:125], v[206:209]
)ASM"
R"ASM(
v_add_u32 v20, s74, v0
buffer_load_dwordx4 v[150:153], v20, s[4:7], s80 offen sc1
s_waitcnt lgkmcnt(3)
v_mfma_f32_16x16x32_bf16 v[194:197], v[242:245], v[118:121], v[194:197]
ds_read_b128 v[226:229], v23 offset:53248
v_mfma_f32_16x16x32_bf16 v[210:213], v[242:245], v[122:125], v[210:213]
s_waitcnt lgkmcnt(3)
v_mfma_f32_16x16x32_bf16 v[198:201], v[246:249], v[118:121], v[198:201]
ds_read_b128 v[230:233], v23 offset:32768
v_mfma_f32_16x16x32_bf16 v[214:217], v[246:249], v[122:125], v[214:217]
buffer_load_dwordx4 v[154:157], v20, s[4:7], s81 offen sc1
s_waitcnt lgkmcnt(3)
v_mfma_f32_16x16x32_bf16 v[202:205], v[250:253], v[118:121], v[202:205]
ds_read_b128 v[234:237], v23 offset:12288
v_mfma_f32_16x16x32_bf16 v[218:221], v[250:253], v[122:125], v[218:221]
s_waitcnt vmcnt(9) lgkmcnt(3)
v_mfma_f32_16x16x32_bf16 v[190:193], v[222:225], v[126:129], v[190:193]
ds_read_b128 v[238:241], v22 offset:13312
s_waitcnt vmcnt(8)
v_mfma_f32_16x16x32_bf16 v[206:209], v[222:225], v[130:133], v[206:209]
buffer_load_dwordx4 v[158:161], v20, s[4:7], s80 offen offset:1024 sc1
s_waitcnt lgkmcnt(3)
v_mfma_f32_16x16x32_bf16 v[194:197], v[226:229], v[126:129], v[194:197]
ds_read_b128 v[242:245], v23 offset:54272
v_mfma_f32_16x16x32_bf16 v[210:213], v[226:229], v[130:133], v[210:213]
s_waitcnt lgkmcnt(3)
v_mfma_f32_16x16x32_bf16 v[198:201], v[230:233], v[126:129], v[198:201]
ds_read_b128 v[246:249], v23 offset:33792
v_mfma_f32_16x16x32_bf16 v[214:217], v[230:233], v[130:133], v[214:217]
buffer_load_dwordx4 v[162:165], v20, s[4:7], s81 offen offset:1024 sc1
s_waitcnt lgkmcnt(3)
v_mfma_f32_16x16x32_bf16 v[202:205], v[234:237], v[126:129], v[202:205]
ds_read_b128 v[250:253], v23 offset:13312
v_mfma_f32_16x16x32_bf16 v[218:221], v[234:237], v[130:133], v[218:221]
s_waitcnt vmcnt(9) lgkmcnt(3)
v_mfma_f32_16x16x32_bf16 v[190:193], v[238:241], v[134:137], v[190:193]
ds_read_b128 v[222:225], v22 offset:14336
s_waitcnt vmcnt(8)
v_mfma_f32_16x16x32_bf16 v[206:209], v[238:241], v[138:141], v[206:209]
buffer_load_dwordx4 v[166:169], v20, s[4:7], s80 offen offset:2048 sc1
s_waitcnt lgkmcnt(3)
v_mfma_f32_16x16x32_bf16 v[194:197], v[242:245], v[134:137], v[194:197]
ds_read_b128 v[226:229], v23 offset:55296
v_mfma_f32_16x16x32_bf16 v[210:213], v[242:245], v[138:141], v[210:213]
s_waitcnt lgkmcnt(3)
v_mfma_f32_16x16x32_bf16 v[198:201], v[246:249], v[134:137], v[198:201]
ds_read_b128 v[230:233], v23 offset:34816
v_mfma_f32_16x16x32_bf16 v[214:217], v[246:249], v[138:141], v[214:217]
buffer_load_dwordx4 v[170:173], v20, s[4:7], s81 offen offset:2048 sc1
s_waitcnt lgkmcnt(3)
v_mfma_f32_16x16x32_bf16 v[202:205], v[250:253], v[134:137], v[202:205]
ds_read_b128 v[234:237], v23 offset:14336
v_mfma_f32_16x16x32_bf16 v[218:221], v[250:253], v[138:141], v[218:221]
s_waitcnt vmcnt(8) lgkmcnt(3)
v_mfma_f32_16x16x32_bf16 v[190:193], v[222:225], v[142:145], v[190:193]
ds_read_b128 v[238:241], v22 offset:15360
s_waitcnt vmcnt(6)
v_mfma_f32_16x16x32_bf16 v[206:209], v[222:225], v[146:149], v[206:209]
buffer_load_dwordx4 v[174:177], v20, s[4:7], s80 offen offset:3072 sc1
s_waitcnt lgkmcnt(3)
v_mfma_f32_16x16x32_bf16 v[194:197], v[226:229], v[142:145], v[194:197]
ds_read_b128 v[242:245], v23 offset:56320
v_mfma_f32_16x16x32_bf16 v[210:213], v[226:229], v[146:149], v[210:213]
s_waitcnt lgkmcnt(3)
v_mfma_f32_16x16x32_bf16 v[198:201], v[230:233], v[142:145], v[198:201]
ds_read_b128 v[246:249], v23 offset:35840
v_mfma_f32_16x16x32_bf16 v[214:217], v[230:233], v[146:149], v[214:217]
buffer_load_dwordx4 v[178:181], v20, s[4:7], s81 offen offset:3072 sc1
s_waitcnt lgkmcnt(3)
v_mfma_f32_16x16x32_bf16 v[202:205], v[234:237], v[142:145], v[202:205]
ds_read_b128 v[250:253], v23 offset:15360
v_mfma_f32_16x16x32_bf16 v[218:221], v[234:237], v[146:149], v[218:221]
s_waitcnt vmcnt(7) lgkmcnt(3)
v_mfma_f32_16x16x32_bf16 v[190:193], v[238:241], v[150:153], v[190:193]
ds_read_b128 v[222:225], v22 offset:16384
s_waitcnt vmcnt(6)
v_mfma_f32_16x16x32_bf16 v[206:209], v[238:241], v[154:157], v[206:209]
v_add_u32 v21, s74, v1
v_add_u32 v21, 0x1000, v21
buffer_load_dwordx2 v[182:183], v21, s[4:7], s80 offen sc1
buffer_load_dwordx2 v[184:185], v21, s[4:7], s80 offen offset:512 sc1
s_waitcnt lgkmcnt(3)
v_mfma_f32_16x16x32_bf16 v[194:197], v[242:245], v[150:153], v[194:197]
ds_read_b128 v[226:229], v23 offset:57344
v_mfma_f32_16x16x32_bf16 v[210:213], v[242:245], v[154:157], v[210:213]
s_waitcnt lgkmcnt(3)
v_mfma_f32_16x16x32_bf16 v[198:201], v[246:249], v[150:153], v[198:201]
ds_read_b128 v[230:233], v23 offset:36864
v_mfma_f32_16x16x32_bf16 v[214:217], v[246:249], v[154:157], v[214:217]
buffer_load_dwordx2 v[186:187], v21, s[4:7], s81 offen sc1
buffer_load_dwordx2 v[188:189], v21, s[4:7], s81 offen offset:512 sc1
s_waitcnt lgkmcnt(3)
v_mfma_f32_16x16x32_bf16 v[202:205], v[250:253], v[150:153], v[202:205]
ds_read_b128 v[234:237], v23 offset:16384
v_mfma_f32_16x16x32_bf16 v[218:221], v[250:253], v[154:157], v[218:221]
s_waitcnt vmcnt(9) lgkmcnt(3)
v_mfma_f32_16x16x32_bf16 v[190:193], v[222:225], v[158:161], v[190:193]
ds_read_b128 v[238:241], v22 offset:17408
s_waitcnt vmcnt(8)
v_mfma_f32_16x16x32_bf16 v[206:209], v[222:225], v[162:165], v[206:209]
s_waitcnt lgkmcnt(3)
v_mfma_f32_16x16x32_bf16 v[194:197], v[226:229], v[158:161], v[194:197]
ds_read_b128 v[242:245], v23 offset:58368
v_mfma_f32_16x16x32_bf16 v[210:213], v[226:229], v[162:165], v[210:213]
s_waitcnt lgkmcnt(3)
v_mfma_f32_16x16x32_bf16 v[198:201], v[230:233], v[158:161], v[198:201]
ds_read_b128 v[246:249], v23 offset:37888
v_mfma_f32_16x16x32_bf16 v[214:217], v[230:233], v[162:165], v[214:217]
s_waitcnt lgkmcnt(3)
v_mfma_f32_16x16x32_bf16 v[202:205], v[234:237], v[158:161], v[202:205]
ds_read_b128 v[250:253], v23 offset:17408
v_mfma_f32_16x16x32_bf16 v[218:221], v[234:237], v[162:165], v[218:221]
s_waitcnt vmcnt(7) lgkmcnt(3)
v_mfma_f32_16x16x32_bf16 v[190:193], v[238:241], v[166:169], v[190:193]
ds_read_b128 v[222:225], v22 offset:18432
s_waitcnt vmcnt(6)
v_mfma_f32_16x16x32_bf16 v[206:209], v[238:241], v[170:173], v[206:209]
s_waitcnt lgkmcnt(3)
v_mfma_f32_16x16x32_bf16 v[194:197], v[242:245], v[166:169], v[194:197]
ds_read_b128 v[226:229], v23 offset:59392
v_mfma_f32_16x16x32_bf16 v[210:213], v[242:245], v[170:173], v[210:213]
s_waitcnt lgkmcnt(3)
v_mfma_f32_16x16x32_bf16 v[198:201], v[246:249], v[166:169], v[198:201]
ds_read_b128 v[230:233], v23 offset:38912
v_mfma_f32_16x16x32_bf16 v[214:217], v[246:249], v[170:173], v[214:217]
s_waitcnt lgkmcnt(3)
v_mfma_f32_16x16x32_bf16 v[202:205], v[250:253], v[166:169], v[202:205]
ds_read_b128 v[234:237], v23 offset:18432
v_mfma_f32_16x16x32_bf16 v[218:221], v[250:253], v[170:173], v[218:221]
s_waitcnt vmcnt(5) lgkmcnt(3)
v_mfma_f32_16x16x32_bf16 v[190:193], v[222:225], v[174:177], v[190:193]
ds_read_b128 v[238:241], v22 offset:19456
v_lshrrev_b32 v254, 8, v0
s_waitcnt vmcnt(4)
v_mfma_f32_16x16x32_bf16 v[206:209], v[222:225], v[178:181], v[206:209]
v_lshlrev_b32 v254, 4, v254
s_waitcnt lgkmcnt(3)
v_mfma_f32_16x16x32_bf16 v[194:197], v[226:229], v[174:177], v[194:197]
ds_read_b128 v[242:245], v23 offset:60416
v_or_b32 v29, s83, v0
v_mfma_f32_16x16x32_bf16 v[210:213], v[226:229], v[178:181], v[210:213]
s_waitcnt lgkmcnt(3)
v_mfma_f32_16x16x32_bf16 v[198:201], v[230:233], v[174:177], v[198:201]
ds_read_b128 v[246:249], v23 offset:39936
v_mfma_f32_16x16x32_bf16 v[214:217], v[230:233], v[178:181], v[214:217]
s_waitcnt lgkmcnt(3)
v_mfma_f32_16x16x32_bf16 v[202:205], v[234:237], v[174:177], v[202:205]
ds_read_b128 v[250:253], v23 offset:19456
v_mfma_f32_16x16x32_bf16 v[218:221], v[234:237], v[178:181], v[218:221]
s_waitcnt vmcnt(2) lgkmcnt(3)
v_mfma_f32_16x16x32_bf16 v[190:193], v[238:241], v[182:185], v[190:193]
ds_read_b128 v[222:225], v254
s_waitcnt vmcnt(0)
v_mfma_f32_16x16x32_bf16 v[206:209], v[238:241], v[186:189], v[206:209]
s_waitcnt lgkmcnt(3)
v_mfma_f32_16x16x32_bf16 v[194:197], v[242:245], v[182:185], v[194:197]
ds_read_b128 v[230:233], v254 offset:512
v_mfma_f32_16x16x32_bf16 v[210:213], v[242:245], v[186:189], v[210:213]
s_waitcnt lgkmcnt(3)
v_mfma_f32_16x16x32_bf16 v[198:201], v[246:249], v[182:185], v[198:201]
ds_read_b128 v[226:229], v254 offset:64
v_mfma_f32_16x16x32_bf16 v[214:217], v[246:249], v[186:189], v[214:217]
s_waitcnt lgkmcnt(3)
v_mfma_f32_16x16x32_bf16 v[202:205], v[250:253], v[182:185], v[202:205]
ds_read_b128 v[234:237], v254 offset:576
v_mfma_f32_16x16x32_bf16 v[218:221], v[250:253], v[186:189], v[218:221]
s_waitcnt lgkmcnt(3)
v_pk_fma_f32 v[190:191], v[222:223], v[24:25], v[190:191] op_sel_hi:[1,0,1]
v_pk_fma_f32 v[192:193], v[224:225], v[24:25], v[192:193] op_sel_hi:[1,0,1]
s_waitcnt lgkmcnt(2)
v_pk_fma_f32 v[190:191], v[190:191], v[24:25], v[230:231] op_sel:[0,1,0] op_sel_hi:[1,1,1]
buffer_load_dwordx4 a[88:91], v0, s[4:7], s79 offen offset:2048 sc1
v_pk_fma_f32 v[192:193], v[192:193], v[24:25], v[232:233] op_sel:[0,1,0] op_sel_hi:[1,1,1]
v_pk_mul_f32 v[238:239], v[190:191], v[190:191]
v_pk_mul_f32 v[240:241], v[192:193], v[192:193]
v_pk_fma_f32 v[238:239], v[238:239], s[16:17], v[28:29] op_sel_hi:[1,0,0]
v_pk_fma_f32 v[240:241], v[240:241], s[16:17], v[28:29] op_sel_hi:[1,0,0]
v_pk_mul_f32 v[238:239], v[238:239], v[190:191]
v_pk_mul_f32 v[240:241], v[240:241], v[192:193]
v_exp_f32 v238, v238
v_exp_f32 v239, v239
v_exp_f32 v240, v240
v_exp_f32 v241, v241
v_pk_add_f32 v[238:239], v[238:239], 1.0 op_sel_hi:[1,0]
v_pk_add_f32 v[240:241], v[240:241], 1.0 op_sel_hi:[1,0]
v_rcp_f32 v238, v238
v_rcp_f32 v239, v239
v_rcp_f32 v240, v240
buffer_load_dwordx4 a[92:95], v0, s[4:7], s79 offen offset:3072 sc1
v_rcp_f32 v241, v241
v_pk_mul_f32 v[190:191], v[190:191], v[238:239]
v_pk_mul_f32 v[192:193], v[192:193], v[240:241]
v_cvt_pk_bf16_f32 v190, v190, v191
v_cvt_pk_bf16_f32 v191, v192, v193
s_waitcnt lgkmcnt(1)
v_pk_fma_f32 v[194:195], v[226:227], v[24:25], v[194:195] op_sel_hi:[1,0,1]
v_pk_fma_f32 v[196:197], v[228:229], v[24:25], v[196:197] op_sel_hi:[1,0,1]
s_waitcnt lgkmcnt(0)
v_pk_fma_f32 v[194:195], v[194:195], v[24:25], v[234:235] op_sel:[0,1,0] op_sel_hi:[1,1,1]
v_pk_fma_f32 v[196:197], v[196:197], v[24:25], v[236:237] op_sel:[0,1,0] op_sel_hi:[1,1,1]
)ASM"
R"ASM(
v_pk_mul_f32 v[238:239], v[194:195], v[194:195]
v_pk_mul_f32 v[240:241], v[196:197], v[196:197]
v_pk_fma_f32 v[238:239], v[238:239], s[16:17], v[28:29] op_sel_hi:[1,0,0]
v_pk_fma_f32 v[240:241], v[240:241], s[16:17], v[28:29] op_sel_hi:[1,0,0]
v_pk_mul_f32 v[238:239], v[238:239], v[194:195]
v_pk_mul_f32 v[240:241], v[240:241], v[196:197]
v_exp_f32 v238, v238
s_add_u32 s79, s79, 0x1000
buffer_load_dwordx4 a[96:99], v0, s[4:7], s79 offen sc1
v_exp_f32 v239, v239
v_exp_f32 v240, v240
v_exp_f32 v241, v241
v_pk_add_f32 v[238:239], v[238:239], 1.0 op_sel_hi:[1,0]
v_pk_add_f32 v[240:241], v[240:241], 1.0 op_sel_hi:[1,0]
v_rcp_f32 v238, v238
v_rcp_f32 v239, v239
v_rcp_f32 v240, v240
v_rcp_f32 v241, v241
v_pk_mul_f32 v[194:195], v[194:195], v[238:239]
v_pk_mul_f32 v[196:197], v[196:197], v[240:241]
v_cvt_pk_bf16_f32 v192, v194, v195
v_cvt_pk_bf16_f32 v193, v196, v197
buffer_store_dwordx4 v[190:193], v0, s[4:7], s82 offen sc1
ds_read_b128 v[242:245], v254 offset:128
ds_read_b128 v[250:253], v254 offset:640
s_add_u32 s79, s96, 5
s_and_b32 s79, s79, 7
s_mul_i32 s79, s79, 0x1400
s_add_u32 s79, s79, s63
buffer_load_dwordx4 a[100:103], v0, s[4:7], s79 offen sc1
ds_read_b128 v[246:249], v254 offset:192
ds_read_b128 v[194:197], v254 offset:704
v_pk_fma_f32 v[206:207], v[222:223], v[26:27], v[206:207] op_sel_hi:[1,0,1]
v_pk_fma_f32 v[208:209], v[224:225], v[26:27], v[208:209] op_sel_hi:[1,0,1]
v_pk_fma_f32 v[206:207], v[206:207], v[26:27], v[230:231] op_sel:[0,1,0] op_sel_hi:[1,1,1]
v_pk_fma_f32 v[208:209], v[208:209], v[26:27], v[232:233] op_sel:[0,1,0] op_sel_hi:[1,1,1]
v_pk_mul_f32 v[238:239], v[206:207], v[206:207]
v_pk_mul_f32 v[240:241], v[208:209], v[208:209]
v_pk_fma_f32 v[238:239], v[238:239], s[16:17], v[28:29] op_sel_hi:[1,0,0]
v_pk_fma_f32 v[240:241], v[240:241], s[16:17], v[28:29] op_sel_hi:[1,0,0]
v_pk_mul_f32 v[238:239], v[238:239], v[206:207]
v_pk_mul_f32 v[240:241], v[240:241], v[208:209]
v_exp_f32 v238, v238
v_exp_f32 v239, v239
v_exp_f32 v240, v240
v_exp_f32 v241, v241
buffer_load_dwordx4 a[104:107], v0, s[4:7], s79 offen offset:1024 sc1
v_pk_add_f32 v[238:239], v[238:239], 1.0 op_sel_hi:[1,0]
v_pk_add_f32 v[240:241], v[240:241], 1.0 op_sel_hi:[1,0]
v_rcp_f32 v238, v238
v_rcp_f32 v239, v239
v_rcp_f32 v240, v240
v_rcp_f32 v241, v241
v_pk_mul_f32 v[206:207], v[206:207], v[238:239]
v_pk_mul_f32 v[208:209], v[208:209], v[240:241]
v_cvt_pk_bf16_f32 v206, v206, v207
v_cvt_pk_bf16_f32 v207, v208, v209
v_pk_fma_f32 v[210:211], v[226:227], v[26:27], v[210:211] op_sel_hi:[1,0,1]
v_pk_fma_f32 v[212:213], v[228:229], v[26:27], v[212:213] op_sel_hi:[1,0,1]
v_pk_fma_f32 v[210:211], v[210:211], v[26:27], v[234:235] op_sel:[0,1,0] op_sel_hi:[1,1,1]
v_pk_fma_f32 v[212:213], v[212:213], v[26:27], v[236:237] op_sel:[0,1,0] op_sel_hi:[1,1,1]
v_pk_mul_f32 v[238:239], v[210:211], v[210:211]
v_pk_mul_f32 v[240:241], v[212:213], v[212:213]
buffer_load_dwordx4 a[108:111], v0, s[4:7], s79 offen offset:2048 sc1
v_pk_fma_f32 v[238:239], v[238:239], s[16:17], v[28:29] op_sel_hi:[1,0,0]
v_pk_fma_f32 v[240:241], v[240:241], s[16:17], v[28:29] op_sel_hi:[1,0,0]
v_pk_mul_f32 v[238:239], v[238:239], v[210:211]
v_pk_mul_f32 v[240:241], v[240:241], v[212:213]
v_exp_f32 v238, v238
v_exp_f32 v239, v239
v_exp_f32 v240, v240
v_exp_f32 v241, v241
v_pk_add_f32 v[238:239], v[238:239], 1.0 op_sel_hi:[1,0]
v_pk_add_f32 v[240:241], v[240:241], 1.0 op_sel_hi:[1,0]
v_rcp_f32 v238, v238
v_rcp_f32 v239, v239
v_rcp_f32 v240, v240
v_rcp_f32 v241, v241
v_pk_mul_f32 v[210:211], v[210:211], v[238:239]
v_pk_mul_f32 v[212:213], v[212:213], v[240:241]
buffer_load_dwordx4 a[112:115], v0, s[4:7], s79 offen offset:3072 sc1
v_cvt_pk_bf16_f32 v208, v210, v211
v_cvt_pk_bf16_f32 v209, v212, v213
buffer_store_dwordx4 v[206:209], v0, s[4:7], s77 offen sc1
s_waitcnt lgkmcnt(3)
v_pk_fma_f32 v[198:199], v[242:243], v[24:25], v[198:199] op_sel_hi:[1,0,1]
v_pk_fma_f32 v[200:201], v[244:245], v[24:25], v[200:201] op_sel_hi:[1,0,1]
s_waitcnt lgkmcnt(2)
v_pk_fma_f32 v[198:199], v[198:199], v[24:25], v[250:251] op_sel:[0,1,0] op_sel_hi:[1,1,1]
v_pk_fma_f32 v[200:201], v[200:201], v[24:25], v[252:253] op_sel:[0,1,0] op_sel_hi:[1,1,1]
v_pk_mul_f32 v[238:239], v[198:199], v[198:199]
v_pk_mul_f32 v[240:241], v[200:201], v[200:201]
v_pk_fma_f32 v[238:239], v[238:239], s[16:17], v[28:29] op_sel_hi:[1,0,0]
v_pk_fma_f32 v[240:241], v[240:241], s[16:17], v[28:29] op_sel_hi:[1,0,0]
v_pk_mul_f32 v[238:239], v[238:239], v[198:199]
v_pk_mul_f32 v[240:241], v[240:241], v[200:201]
v_exp_f32 v238, v238
v_exp_f32 v239, v239
v_exp_f32 v240, v240
s_add_u32 s79, s79, 0x1000
buffer_load_dwordx4 a[116:119], v0, s[4:7], s79 offen sc1
v_exp_f32 v241, v241
v_pk_add_f32 v[238:239], v[238:239], 1.0 op_sel_hi:[1,0]
v_pk_add_f32 v[240:241], v[240:241], 1.0 op_sel_hi:[1,0]
v_rcp_f32 v238, v238
v_rcp_f32 v239, v239
v_rcp_f32 v240, v240
v_rcp_f32 v241, v241
v_pk_mul_f32 v[198:199], v[198:199], v[238:239]
v_pk_mul_f32 v[200:201], v[200:201], v[240:241]
v_cvt_pk_bf16_f32 v198, v198, v199
v_cvt_pk_bf16_f32 v199, v200, v201
s_waitcnt lgkmcnt(1)
v_pk_fma_f32 v[202:203], v[246:247], v[24:25], v[202:203] op_sel_hi:[1,0,1]
v_pk_fma_f32 v[204:205], v[248:249], v[24:25], v[204:205] op_sel_hi:[1,0,1]
s_waitcnt lgkmcnt(0)
v_pk_fma_f32 v[202:203], v[202:203], v[24:25], v[194:195] op_sel:[0,1,0] op_sel_hi:[1,1,1]
v_pk_fma_f32 v[204:205], v[204:205], v[24:25], v[196:197] op_sel:[0,1,0] op_sel_hi:[1,1,1]
v_pk_mul_f32 v[238:239], v[202:203], v[202:203]
s_add_u32 s79, s96, 6
s_and_b32 s79, s79, 7
s_mul_i32 s79, s79, 0x1400
s_add_u32 s79, s79, s63
buffer_load_dwordx4 a[120:123], v0, s[4:7], s79 offen sc1
v_pk_mul_f32 v[240:241], v[204:205], v[204:205]
v_pk_fma_f32 v[238:239], v[238:239], s[16:17], v[28:29] op_sel_hi:[1,0,0]
v_pk_fma_f32 v[240:241], v[240:241], s[16:17], v[28:29] op_sel_hi:[1,0,0]
v_pk_mul_f32 v[238:239], v[238:239], v[202:203]
v_pk_mul_f32 v[240:241], v[240:241], v[204:205]
v_exp_f32 v238, v238
v_exp_f32 v239, v239
v_exp_f32 v240, v240
v_exp_f32 v241, v241
v_pk_add_f32 v[238:239], v[238:239], 1.0 op_sel_hi:[1,0]
v_pk_add_f32 v[240:241], v[240:241], 1.0 op_sel_hi:[1,0]
v_rcp_f32 v238, v238
v_rcp_f32 v239, v239
v_rcp_f32 v240, v240
v_rcp_f32 v241, v241
v_pk_mul_f32 v[202:203], v[202:203], v[238:239]
buffer_load_dwordx4 a[124:127], v0, s[4:7], s79 offen offset:1024 sc1
v_pk_mul_f32 v[204:205], v[204:205], v[240:241]
v_cvt_pk_bf16_f32 v200, v202, v203
v_cvt_pk_bf16_f32 v201, v204, v205
buffer_store_dwordx4 v[198:201], v0, s[4:7], s82 offen offset:1024 sc1
v_pk_fma_f32 v[214:215], v[242:243], v[26:27], v[214:215] op_sel_hi:[1,0,1]
v_pk_fma_f32 v[216:217], v[244:245], v[26:27], v[216:217] op_sel_hi:[1,0,1]
v_pk_fma_f32 v[214:215], v[214:215], v[26:27], v[250:251] op_sel:[0,1,0] op_sel_hi:[1,1,1]
v_pk_fma_f32 v[216:217], v[216:217], v[26:27], v[252:253] op_sel:[0,1,0] op_sel_hi:[1,1,1]
v_pk_mul_f32 v[238:239], v[214:215], v[214:215]
v_pk_mul_f32 v[240:241], v[216:217], v[216:217]
v_pk_fma_f32 v[238:239], v[238:239], s[16:17], v[28:29] op_sel_hi:[1,0,0]
v_pk_fma_f32 v[240:241], v[240:241], s[16:17], v[28:29] op_sel_hi:[1,0,0]
v_pk_mul_f32 v[238:239], v[238:239], v[214:215]
v_pk_mul_f32 v[240:241], v[240:241], v[216:217]
v_exp_f32 v238, v238
v_exp_f32 v239, v239
buffer_load_dwordx4 a[128:131], v0, s[4:7], s79 offen offset:2048 sc1
v_exp_f32 v240, v240
v_exp_f32 v241, v241
v_pk_add_f32 v[238:239], v[238:239], 1.0 op_sel_hi:[1,0]
v_pk_add_f32 v[240:241], v[240:241], 1.0 op_sel_hi:[1,0]
v_rcp_f32 v238, v238
v_rcp_f32 v239, v239
v_rcp_f32 v240, v240
v_rcp_f32 v241, v241
v_pk_mul_f32 v[214:215], v[214:215], v[238:239]
v_pk_mul_f32 v[216:217], v[216:217], v[240:241]
v_cvt_pk_bf16_f32 v214, v214, v215
v_cvt_pk_bf16_f32 v215, v216, v217
v_add_u32 v22, 0x5000, v0
ds_read_b128 v[222:225], v22 offset:40960
ds_read_b128 v[226:229], v22 offset:20480
ds_read_b128 v[230:233], v22
buffer_load_dwordx4 a[132:135], v0, s[4:7], s79 offen offset:3072 sc1
v_pk_fma_f32 v[218:219], v[246:247], v[26:27], v[218:219] op_sel_hi:[1,0,1]
v_pk_fma_f32 v[220:221], v[248:249], v[26:27], v[220:221] op_sel_hi:[1,0,1]
v_pk_fma_f32 v[218:219], v[218:219], v[26:27], v[194:195] op_sel:[0,1,0] op_sel_hi:[1,1,1]
v_pk_fma_f32 v[220:221], v[220:221], v[26:27], v[196:197] op_sel:[0,1,0] op_sel_hi:[1,1,1]
v_pk_mul_f32 v[238:239], v[218:219], v[218:219]
v_pk_mul_f32 v[240:241], v[220:221], v[220:221]
v_pk_fma_f32 v[238:239], v[238:239], s[16:17], v[28:29] op_sel_hi:[1,0,0]
v_pk_fma_f32 v[240:241], v[240:241], s[16:17], v[28:29] op_sel_hi:[1,0,0]
v_pk_mul_f32 v[238:239], v[238:239], v[218:219]
v_pk_mul_f32 v[240:241], v[240:241], v[220:221]
v_exp_f32 v238, v238
v_exp_f32 v239, v239
v_exp_f32 v240, v240
v_exp_f32 v241, v241
v_pk_add_f32 v[238:239], v[238:239], 1.0 op_sel_hi:[1,0]
v_pk_add_f32 v[240:241], v[240:241], 1.0 op_sel_hi:[1,0]
v_rcp_f32 v238, v238
v_rcp_f32 v239, v239
v_rcp_f32 v240, v240
v_rcp_f32 v241, v241
v_pk_mul_f32 v[218:219], v[218:219], v[238:239]
v_pk_mul_f32 v[220:221], v[220:221], v[240:241]
v_cvt_pk_bf16_f32 v216, v218, v219
v_cvt_pk_bf16_f32 v217, v220, v221
buffer_store_dwordx4 v[214:217], v0, s[4:7], s77 offen offset:1024 sc1
)ASM"
R"ASM(
s_waitcnt lgkmcnt(2)
v_mfma_f32_16x16x32_bf16 v[194:197], v[222:225], v[30:33], 0
ds_read_b128 v[238:241], v22 offset:41984
v_mfma_f32_16x16x32_bf16 v[210:213], v[222:225], v[34:37], 0
s_add_u32 s79, s79, 0x1000
buffer_load_dwordx4 a[136:139], v0, s[4:7], s79 offen sc1
s_waitcnt lgkmcnt(2)
v_mfma_f32_16x16x32_bf16 v[234:237], v[226:229], v[30:33], 0
ds_read_b128 v[242:245], v22 offset:21504
v_mfma_f32_16x16x32_bf16 v[250:253], v[226:229], v[34:37], 0
s_waitcnt lgkmcnt(2)
v_mfma_f32_16x16x32_bf16 v[202:205], v[230:233], v[30:33], 0
ds_read_b128 v[246:249], v22 offset:1024
v_mfma_f32_16x16x32_bf16 v[218:221], v[230:233], v[34:37], 0
s_waitcnt lgkmcnt(2)
v_mfma_f32_16x16x32_bf16 v[194:197], v[238:241], v[38:41], v[194:197]
ds_read_b128 v[222:225], v22 offset:43008
v_mfma_f32_16x16x32_bf16 v[210:213], v[238:241], v[42:45], v[210:213]
s_add_u32 s79, s96, 7
s_and_b32 s79, s79, 7
s_mul_i32 s79, s79, 0x1400
s_add_u32 s79, s79, s63
buffer_load_dwordx4 a[140:143], v0, s[4:7], s79 offen sc1
s_waitcnt lgkmcnt(2)
v_mfma_f32_16x16x32_bf16 v[234:237], v[242:245], v[38:41], v[234:237]
ds_read_b128 v[226:229], v22 offset:22528
v_mfma_f32_16x16x32_bf16 v[250:253], v[242:245], v[42:45], v[250:253]
s_waitcnt lgkmcnt(2)
v_mfma_f32_16x16x32_bf16 v[202:205], v[246:249], v[38:41], v[202:205]
ds_read_b128 v[230:233], v22 offset:2048
v_mfma_f32_16x16x32_bf16 v[218:221], v[246:249], v[42:45], v[218:221]
buffer_load_dwordx4 a[144:147], v0, s[4:7], s79 offen offset:1024 sc1
s_waitcnt lgkmcnt(2)
v_mfma_f32_16x16x32_bf16 v[194:197], v[222:225], v[46:49], v[194:197]
ds_read_b128 v[238:241], v22 offset:44032
v_mfma_f32_16x16x32_bf16 v[210:213], v[222:225], v[50:53], v[210:213]
s_waitcnt lgkmcnt(2)
v_mfma_f32_16x16x32_bf16 v[234:237], v[226:229], v[46:49], v[234:237]
ds_read_b128 v[242:245], v22 offset:23552
v_mfma_f32_16x16x32_bf16 v[250:253], v[226:229], v[50:53], v[250:253]
buffer_load_dwordx4 a[148:151], v0, s[4:7], s79 offen offset:2048 sc1
s_waitcnt lgkmcnt(2)
v_mfma_f32_16x16x32_bf16 v[202:205], v[230:233], v[46:49], v[202:205]
ds_read_b128 v[246:249], v22 offset:3072
v_mfma_f32_16x16x32_bf16 v[218:221], v[230:233], v[50:53], v[218:221]
s_waitcnt lgkmcnt(2)
v_mfma_f32_16x16x32_bf16 v[194:197], v[238:241], v[54:57], v[194:197]
ds_read_b128 v[222:225], v22 offset:45056
v_mfma_f32_16x16x32_bf16 v[210:213], v[238:241], v[58:61], v[210:213]
s_waitcnt lgkmcnt(2)
v_mfma_f32_16x16x32_bf16 v[234:237], v[242:245], v[54:57], v[234:237]
ds_read_b128 v[226:229], v22 offset:24576
v_mfma_f32_16x16x32_bf16 v[250:253], v[242:245], v[58:61], v[250:253]
buffer_load_dwordx4 a[152:155], v0, s[4:7], s79 offen offset:3072 sc1
s_waitcnt lgkmcnt(2)
v_mfma_f32_16x16x32_bf16 v[202:205], v[246:249], v[54:57], v[202:205]
ds_read_b128 v[230:233], v22 offset:4096
v_mfma_f32_16x16x32_bf16 v[218:221], v[246:249], v[58:61], v[218:221]
s_waitcnt lgkmcnt(2)
v_mfma_f32_16x16x32_bf16 v[194:197], v[222:225], v[62:65], v[194:197]
ds_read_b128 v[238:241], v22 offset:46080
v_mfma_f32_16x16x32_bf16 v[210:213], v[222:225], v[66:69], v[210:213]
s_add_u32 s79, s79, 0x1000
buffer_load_dwordx4 a[156:159], v0, s[4:7], s79 offen sc1
s_waitcnt lgkmcnt(2)
v_mfma_f32_16x16x32_bf16 v[234:237], v[226:229], v[62:65], v[234:237]
ds_read_b128 v[242:245], v22 offset:25600
v_mfma_f32_16x16x32_bf16 v[250:253], v[226:229], v[66:69], v[250:253]
s_waitcnt lgkmcnt(2)
v_mfma_f32_16x16x32_bf16 v[202:205], v[230:233], v[62:65], v[202:205]
ds_read_b128 v[246:249], v22 offset:5120
v_mfma_f32_16x16x32_bf16 v[218:221], v[230:233], v[66:69], v[218:221]
s_add_u32 s79, s96, 8
s_and_b32 s79, s79, 3
s_or_b32 s79, s79, 8
s_mul_i32 s79, s79, 0x1400
s_add_u32 s79, s79, s63
buffer_load_dwordx4 a[160:163], v0, s[4:7], s79 offen sc1
s_waitcnt lgkmcnt(2)
v_mfma_f32_16x16x32_bf16 v[194:197], v[238:241], v[70:73], v[194:197]
ds_read_b128 v[222:225], v22 offset:47104
v_mfma_f32_16x16x32_bf16 v[210:213], v[238:241], v[74:77], v[210:213]
s_waitcnt lgkmcnt(2)
v_mfma_f32_16x16x32_bf16 v[234:237], v[242:245], v[70:73], v[234:237]
ds_read_b128 v[226:229], v22 offset:26624
v_mfma_f32_16x16x32_bf16 v[250:253], v[242:245], v[74:77], v[250:253]
s_waitcnt lgkmcnt(2)
v_mfma_f32_16x16x32_bf16 v[202:205], v[246:249], v[70:73], v[202:205]
ds_read_b128 v[230:233], v22 offset:6144
v_mfma_f32_16x16x32_bf16 v[218:221], v[246:249], v[74:77], v[218:221]
buffer_load_dwordx4 a[164:167], v0, s[4:7], s79 offen offset:1024 sc1
s_waitcnt lgkmcnt(2)
v_mfma_f32_16x16x32_bf16 v[194:197], v[222:225], v[78:81], v[194:197]
ds_read_b128 v[238:241], v22 offset:48128
v_mfma_f32_16x16x32_bf16 v[210:213], v[222:225], v[82:85], v[210:213]
s_waitcnt lgkmcnt(2)
v_mfma_f32_16x16x32_bf16 v[234:237], v[226:229], v[78:81], v[234:237]
ds_read_b128 v[242:245], v22 offset:27648
v_mfma_f32_16x16x32_bf16 v[250:253], v[226:229], v[82:85], v[250:253]
buffer_load_dwordx4 a[168:171], v0, s[4:7], s79 offen offset:2048 sc1
s_waitcnt lgkmcnt(2)
v_mfma_f32_16x16x32_bf16 v[202:205], v[230:233], v[78:81], v[202:205]
ds_read_b128 v[246:249], v22 offset:7168
v_mfma_f32_16x16x32_bf16 v[218:221], v[230:233], v[82:85], v[218:221]
s_waitcnt lgkmcnt(2)
v_mfma_f32_16x16x32_bf16 v[194:197], v[238:241], v[86:89], v[194:197]
ds_read_b128 v[222:225], v22 offset:49152
v_mfma_f32_16x16x32_bf16 v[210:213], v[238:241], v[90:93], v[210:213]
buffer_load_dwordx4 a[172:175], v0, s[4:7], s79 offen offset:3072 sc1
s_waitcnt lgkmcnt(2)
v_mfma_f32_16x16x32_bf16 v[234:237], v[242:245], v[86:89], v[234:237]
ds_read_b128 v[226:229], v22 offset:28672
v_mfma_f32_16x16x32_bf16 v[250:253], v[242:245], v[90:93], v[250:253]
s_waitcnt lgkmcnt(2)
v_mfma_f32_16x16x32_bf16 v[202:205], v[246:249], v[86:89], v[202:205]
ds_read_b128 v[230:233], v22 offset:8192
v_mfma_f32_16x16x32_bf16 v[218:221], v[246:249], v[90:93], v[218:221]
s_add_u32 s44, s84, 0x4000
s_waitcnt vmcnt(10)
s_barrier
buffer_store_dwordx2 v[4:5], v2, s[4:7], s44 offen sc1
s_waitcnt lgkmcnt(2)
v_mfma_f32_16x16x32_bf16 v[194:197], v[222:225], v[94:97], v[194:197]
ds_read_b128 v[238:241], v22 offset:50176
v_mfma_f32_16x16x32_bf16 v[210:213], v[222:225], v[98:101], v[210:213]
s_add_u32 s79, s79, 0x1000
buffer_load_dwordx4 a[176:179], v0, s[4:7], s79 offen sc1
s_waitcnt lgkmcnt(2)
v_mfma_f32_16x16x32_bf16 v[234:237], v[226:229], v[94:97], v[234:237]
ds_read_b128 v[242:245], v22 offset:29696
v_mfma_f32_16x16x32_bf16 v[250:253], v[226:229], v[98:101], v[250:253]
s_waitcnt lgkmcnt(2)
v_mfma_f32_16x16x32_bf16 v[202:205], v[230:233], v[94:97], v[202:205]
ds_read_b128 v[246:249], v22 offset:9216
v_mfma_f32_16x16x32_bf16 v[218:221], v[230:233], v[98:101], v[218:221]
s_add_u32 s79, s96, 9
s_and_b32 s79, s79, 3
s_or_b32 s79, s79, 8
s_mul_i32 s79, s79, 0x1400
s_add_u32 s79, s79, s63
buffer_load_dwordx4 a[180:183], v0, s[4:7], s79 offen sc1
s_waitcnt lgkmcnt(2)
v_mfma_f32_16x16x32_bf16 v[194:197], v[238:241], v[102:105], v[194:197]
ds_read_b128 v[222:225], v22 offset:51200
v_mfma_f32_16x16x32_bf16 v[210:213], v[238:241], v[106:109], v[210:213]
s_waitcnt lgkmcnt(2)
v_mfma_f32_16x16x32_bf16 v[234:237], v[242:245], v[102:105], v[234:237]
ds_read_b128 v[226:229], v22 offset:30720
v_mfma_f32_16x16x32_bf16 v[250:253], v[242:245], v[106:109], v[250:253]
buffer_load_dwordx4 a[184:187], v0, s[4:7], s79 offen offset:1024 sc1
s_waitcnt lgkmcnt(2)
v_mfma_f32_16x16x32_bf16 v[202:205], v[246:249], v[102:105], v[202:205]
ds_read_b128 v[230:233], v22 offset:10240
v_mfma_f32_16x16x32_bf16 v[218:221], v[246:249], v[106:109], v[218:221]
s_waitcnt lgkmcnt(2)
v_mfma_f32_16x16x32_bf16 v[194:197], v[222:225], v[110:113], v[194:197]
ds_read_b128 v[238:241], v22 offset:52224
v_mfma_f32_16x16x32_bf16 v[210:213], v[222:225], v[114:117], v[210:213]
buffer_load_dwordx4 a[188:191], v0, s[4:7], s79 offen offset:2048 sc1
s_waitcnt lgkmcnt(2)
v_mfma_f32_16x16x32_bf16 v[234:237], v[226:229], v[110:113], v[234:237]
ds_read_b128 v[242:245], v22 offset:31744
v_mfma_f32_16x16x32_bf16 v[250:253], v[226:229], v[114:117], v[250:253]
s_waitcnt lgkmcnt(2)
v_mfma_f32_16x16x32_bf16 v[202:205], v[230:233], v[110:113], v[202:205]
ds_read_b128 v[246:249], v22 offset:11264
v_mfma_f32_16x16x32_bf16 v[218:221], v[230:233], v[114:117], v[218:221]
s_waitcnt lgkmcnt(2)
v_mfma_f32_16x16x32_bf16 v[194:197], v[238:241], v[118:121], v[194:197]
ds_read_b128 v[222:225], v22 offset:53248
v_mfma_f32_16x16x32_bf16 v[210:213], v[238:241], v[122:125], v[210:213]
buffer_load_dwordx4 a[192:195], v0, s[4:7], s79 offen offset:3072 sc1
s_waitcnt lgkmcnt(2)
v_mfma_f32_16x16x32_bf16 v[234:237], v[242:245], v[118:121], v[234:237]
ds_read_b128 v[226:229], v22 offset:32768
v_mfma_f32_16x16x32_bf16 v[250:253], v[242:245], v[122:125], v[250:253]
s_waitcnt lgkmcnt(2)
v_mfma_f32_16x16x32_bf16 v[202:205], v[246:249], v[118:121], v[202:205]
ds_read_b128 v[230:233], v22 offset:12288
v_mfma_f32_16x16x32_bf16 v[218:221], v[246:249], v[122:125], v[218:221]
s_add_u32 s79, s79, 0x1000
buffer_load_dwordx4 a[196:199], v0, s[4:7], s79 offen sc1
s_waitcnt lgkmcnt(2)
v_mfma_f32_16x16x32_bf16 v[194:197], v[222:225], v[126:129], v[194:197]
ds_read_b128 v[238:241], v22 offset:54272
v_mfma_f32_16x16x32_bf16 v[210:213], v[222:225], v[130:133], v[210:213]
s_waitcnt lgkmcnt(2)
v_mfma_f32_16x16x32_bf16 v[234:237], v[226:229], v[126:129], v[234:237]
ds_read_b128 v[242:245], v22 offset:33792
v_mfma_f32_16x16x32_bf16 v[250:253], v[226:229], v[130:133], v[250:253]
s_add_u32 s79, s96, 10
s_and_b32 s79, s79, 3
s_or_b32 s79, s79, 8
s_mul_i32 s79, s79, 0x1400
s_add_u32 s79, s79, s63
buffer_load_dwordx4 a[200:203], v0, s[4:7], s79 offen sc1
s_waitcnt lgkmcnt(2)
v_mfma_f32_16x16x32_bf16 v[202:205], v[230:233], v[126:129], v[202:205]
ds_read_b128 v[246:249], v22 offset:13312
v_mfma_f32_16x16x32_bf16 v[218:221], v[230:233], v[130:133], v[218:221]
)ASM"
R"ASM(
ds_read_b128 v[30:33], v254 offset:256
ds_read_b128 v[42:45], v254 offset:768
ds_read_b128 v[34:37], v254 offset:320
ds_read_b128 v[46:49], v254 offset:832
ds_read_b128 v[38:41], v254 offset:384
ds_read_b128 v[50:53], v254 offset:896
s_and_b32 s46, s38, 3
s_add_u32 s45, s46, 8
s_lshl_b32 s45, s45, 10
s_add_u32 s87, s18, s45
s_add_u32 s88, s87, 0xe000
s_lshr_b32 s45, s46, 1
s_waitcnt lgkmcnt(8)
v_mfma_f32_16x16x32_bf16 v[194:197], v[238:241], v[134:137], v[194:197]
ds_read_b128 v[222:225], v22 offset:55296
v_mfma_f32_16x16x32_bf16 v[210:213], v[238:241], v[138:141], v[210:213]
s_waitcnt lgkmcnt(8)
v_mfma_f32_16x16x32_bf16 v[234:237], v[242:245], v[134:137], v[234:237]
ds_read_b128 v[226:229], v22 offset:34816
v_mfma_f32_16x16x32_bf16 v[250:253], v[242:245], v[138:141], v[250:253]
buffer_load_dwordx4 a[204:207], v0, s[4:7], s79 offen offset:1024 sc1
s_waitcnt lgkmcnt(8)
v_mfma_f32_16x16x32_bf16 v[202:205], v[246:249], v[134:137], v[202:205]
ds_read_b128 v[230:233], v22 offset:14336
v_mfma_f32_16x16x32_bf16 v[218:221], v[246:249], v[138:141], v[218:221]
s_lshl_b32 s43, s37, 7
s_lshl_b32 s44, s34, 5
s_add_u32 s43, s43, s44
v_and_b32 v3, 24, v1
v_add_u32 v3, s43, v3
s_mov_b32 s86, 0x4000
s_lshl_b32 s45, s45, 10
s_and_b32 s46, s46, 1
s_lshl_b32 s46, s46, 9
s_add_u32 s46, s46, s45
s_add_u32 s46, s46, 0x3000
s_add_u32 s89, s18, s46
s_waitcnt lgkmcnt(2)
v_mfma_f32_16x16x32_bf16 v[194:197], v[222:225], v[142:145], v[194:197]
ds_read_b128 v[238:241], v22 offset:56320
v_mfma_f32_16x16x32_bf16 v[210:213], v[222:225], v[146:149], v[210:213]
buffer_load_dwordx4 a[208:211], v0, s[4:7], s79 offen offset:2048 sc1
s_waitcnt lgkmcnt(2)
v_mfma_f32_16x16x32_bf16 v[234:237], v[226:229], v[142:145], v[234:237]
ds_read_b128 v[242:245], v22 offset:35840
v_mfma_f32_16x16x32_bf16 v[250:253], v[226:229], v[146:149], v[250:253]
s_waitcnt lgkmcnt(2)
v_mfma_f32_16x16x32_bf16 v[202:205], v[230:233], v[142:145], v[202:205]
ds_read_b128 v[246:249], v22 offset:15360
v_mfma_f32_16x16x32_bf16 v[218:221], v[230:233], v[146:149], v[218:221]
buffer_load_dwordx4 a[212:215], v0, s[4:7], s79 offen offset:3072 sc1
s_add_u32 s19, s89, 0xe000
s_cmp_lt_u32 s38, 12
s_cselect_b32 s45, 0, 0x80000000
v_or_b32 v21, s45, v1
s_mul_i32 s46, s34, 0x3800
s_mul_i32 s45, s35, 0x38000
s_add_u32 s46, s46, s45
s_waitcnt lgkmcnt(2)
v_mfma_f32_16x16x32_bf16 v[194:197], v[238:241], v[150:153], v[194:197]
ds_read_b128 v[222:225], v22 offset:57344
v_mfma_f32_16x16x32_bf16 v[210:213], v[238:241], v[154:157], v[210:213]
s_waitcnt lgkmcnt(2)
v_mfma_f32_16x16x32_bf16 v[234:237], v[242:245], v[150:153], v[234:237]
ds_read_b128 v[226:229], v22 offset:36864
v_mfma_f32_16x16x32_bf16 v[250:253], v[242:245], v[154:157], v[250:253]
s_waitcnt lgkmcnt(2)
v_mfma_f32_16x16x32_bf16 v[202:205], v[246:249], v[150:153], v[202:205]
ds_read_b128 v[230:233], v22 offset:16384
v_mfma_f32_16x16x32_bf16 v[218:221], v[246:249], v[154:157], v[218:221]
s_add_u32 s79, s79, 0x1000
buffer_load_dwordx4 a[216:219], v0, s[4:7], s79 offen sc1
s_add_u32 s46, s46, 0xd30000
s_add_u32 s45, s34, 1
s_and_b32 s45, s45, 3
s_mul_i32 s45, s45, 0xe000
s_add_u32 s80, s45, s46
s_add_u32 s45, s34, 2
s_waitcnt lgkmcnt(2)
v_mfma_f32_16x16x32_bf16 v[194:197], v[222:225], v[158:161], v[194:197]
ds_read_b128 v[238:241], v22 offset:58368
v_mfma_f32_16x16x32_bf16 v[210:213], v[222:225], v[162:165], v[210:213]
s_waitcnt lgkmcnt(2)
v_mfma_f32_16x16x32_bf16 v[234:237], v[226:229], v[158:161], v[234:237]
ds_read_b128 v[242:245], v22 offset:37888
v_mfma_f32_16x16x32_bf16 v[250:253], v[226:229], v[162:165], v[250:253]
s_add_u32 s79, s96, 11
s_and_b32 s79, s79, 3
s_or_b32 s79, s79, 8
s_mul_i32 s79, s79, 0x1400
s_add_u32 s79, s79, s63
buffer_load_dwordx4 a[220:223], v0, s[4:7], s79 offen sc1
s_waitcnt lgkmcnt(2)
v_mfma_f32_16x16x32_bf16 v[202:205], v[230:233], v[158:161], v[202:205]
ds_read_b128 v[246:249], v22 offset:17408
v_mfma_f32_16x16x32_bf16 v[218:221], v[230:233], v[162:165], v[218:221]
buffer_load_dwordx2 v[58:59], v3, s[4:7], s86 offen sc1
s_and_b32 s45, s45, 3
s_mul_i32 s45, s45, 0xe000
s_add_u32 s81, s45, s46
s_add_u32 s45, s34, 3
s_and_b32 s45, s45, 3
s_mul_i32 s45, s45, 0xe000
s_waitcnt lgkmcnt(2)
v_mfma_f32_16x16x32_bf16 v[194:197], v[238:241], v[166:169], v[194:197]
ds_read_b128 v[222:225], v22 offset:59392
v_mfma_f32_16x16x32_bf16 v[210:213], v[238:241], v[170:173], v[210:213]
buffer_load_dwordx4 a[224:227], v0, s[4:7], s79 offen offset:1024 sc1
s_waitcnt lgkmcnt(2)
v_mfma_f32_16x16x32_bf16 v[234:237], v[242:245], v[166:169], v[234:237]
ds_read_b128 v[226:229], v22 offset:38912
v_mfma_f32_16x16x32_bf16 v[250:253], v[242:245], v[170:173], v[250:253]
s_waitcnt lgkmcnt(2)
v_mfma_f32_16x16x32_bf16 v[202:205], v[246:249], v[166:169], v[202:205]
ds_read_b128 v[230:233], v22 offset:18432
v_mfma_f32_16x16x32_bf16 v[218:221], v[246:249], v[170:173], v[218:221]
s_add_u32 s82, s45, s46
s_add_u32 s45, s34, 4
s_and_b32 s45, s45, 3
s_mul_i32 s45, s45, 0xe000
s_add_u32 s77, s45, s46
s_waitcnt lgkmcnt(2)
v_mfma_f32_16x16x32_bf16 v[194:197], v[222:225], v[174:177], v[194:197]
ds_read_b128 v[238:241], v22 offset:60416
v_mfma_f32_16x16x32_bf16 v[210:213], v[222:225], v[178:181], v[210:213]
buffer_load_dwordx4 a[228:231], v0, s[4:7], s79 offen offset:2048 sc1
s_waitcnt lgkmcnt(2)
v_mfma_f32_16x16x32_bf16 v[234:237], v[226:229], v[174:177], v[234:237]
ds_read_b128 v[242:245], v22 offset:39936
v_mfma_f32_16x16x32_bf16 v[250:253], v[226:229], v[178:181], v[250:253]
s_waitcnt lgkmcnt(2)
v_mfma_f32_16x16x32_bf16 v[202:205], v[230:233], v[174:177], v[202:205]
ds_read_b128 v[246:249], v22 offset:19456
v_mfma_f32_16x16x32_bf16 v[218:221], v[230:233], v[178:181], v[218:221]
buffer_load_dwordx4 a[232:235], v0, s[4:7], s79 offen offset:3072 sc1
s_waitcnt lgkmcnt(2)
v_mfma_f32_16x16x32_bf16 v[194:197], v[238:241], v[182:185], v[194:197]
v_mfma_f32_16x16x32_bf16 v[210:213], v[238:241], v[186:189], v[210:213]
s_waitcnt lgkmcnt(1)
v_mfma_f32_16x16x32_bf16 v[234:237], v[242:245], v[182:185], v[234:237]
v_mfma_f32_16x16x32_bf16 v[250:253], v[242:245], v[186:189], v[250:253]
s_add_u32 s79, s79, 0x1000
buffer_load_dwordx4 a[236:239], v0, s[4:7], s79 offen sc1
s_waitcnt lgkmcnt(0)
v_mfma_f32_16x16x32_bf16 v[202:205], v[246:249], v[182:185], v[202:205]
v_mfma_f32_16x16x32_bf16 v[218:221], v[246:249], v[186:189], v[218:221]
s_waitcnt vmcnt(4)
v_cmp_eq_u64_e64 s[94:95], s[40:41], v[58:59]
s_nop 0
s_cmp_eq_u64 s[94:95], exec
s_cbranch_scc0 .Lk_g1s_13
.Lk_g1b_14:
v_pk_fma_f32 v[194:195], v[30:31], v[24:25], v[194:195] op_sel_hi:[1,0,1]
s_add_u32 s43, s96, 0
s_and_b32 s43, s43, 7
v_lshl_add_u32 v20, s43, 10, v0
buffer_load_dwordx4 v[60:63], v20, s[4:7], s80 offen sc1
v_pk_fma_f32 v[196:197], v[32:33], v[24:25], v[196:197] op_sel_hi:[1,0,1]
v_pk_fma_f32 v[194:195], v[194:195], v[24:25], v[42:43] op_sel:[0,1,0] op_sel_hi:[1,1,1]
v_pk_fma_f32 v[196:197], v[196:197], v[24:25], v[44:45] op_sel:[0,1,0] op_sel_hi:[1,1,1]
v_pk_mul_f32 v[54:55], v[194:195], v[194:195]
v_pk_mul_f32 v[56:57], v[196:197], v[196:197]
v_pk_fma_f32 v[54:55], v[54:55], s[16:17], v[28:29] op_sel_hi:[1,0,0]
buffer_load_dwordx4 v[64:67], v20, s[4:7], s81 offen sc1
v_pk_fma_f32 v[56:57], v[56:57], s[16:17], v[28:29] op_sel_hi:[1,0,0]
v_pk_mul_f32 v[54:55], v[54:55], v[194:195]
v_pk_mul_f32 v[56:57], v[56:57], v[196:197]
v_exp_f32 v54, v54
v_exp_f32 v55, v55
v_exp_f32 v56, v56
buffer_load_dwordx4 v[68:71], v20, s[4:7], s82 offen sc1
v_exp_f32 v57, v57
v_pk_add_f32 v[54:55], v[54:55], 1.0 op_sel_hi:[1,0]
v_pk_add_f32 v[56:57], v[56:57], 1.0 op_sel_hi:[1,0]
v_rcp_f32 v54, v54
v_rcp_f32 v55, v55
buffer_load_dwordx4 v[72:75], v20, s[4:7], s77 offen sc1
v_rcp_f32 v56, v56
v_rcp_f32 v57, v57
v_pk_mul_f32 v[194:195], v[194:195], v[54:55]
v_pk_mul_f32 v[196:197], v[196:197], v[56:57]
v_cvt_pk_bf16_f32 v112, v194, v195
v_cvt_pk_bf16_f32 v113, v196, v197
s_add_u32 s43, s96, 1
s_and_b32 s43, s43, 7
v_lshl_add_u32 v22, s43, 10, v0
buffer_load_dwordx4 v[76:79], v22, s[4:7], s80 offen sc1
v_pk_fma_f32 v[234:235], v[34:35], v[24:25], v[234:235] op_sel_hi:[1,0,1]
v_pk_fma_f32 v[236:237], v[36:37], v[24:25], v[236:237] op_sel_hi:[1,0,1]
v_pk_fma_f32 v[234:235], v[234:235], v[24:25], v[46:47] op_sel:[0,1,0] op_sel_hi:[1,1,1]
v_pk_fma_f32 v[236:237], v[236:237], v[24:25], v[48:49] op_sel:[0,1,0] op_sel_hi:[1,1,1]
v_pk_mul_f32 v[54:55], v[234:235], v[234:235]
v_pk_mul_f32 v[56:57], v[236:237], v[236:237]
buffer_load_dwordx4 v[80:83], v22, s[4:7], s81 offen sc1
v_pk_fma_f32 v[54:55], v[54:55], s[16:17], v[28:29] op_sel_hi:[1,0,0]
v_pk_fma_f32 v[56:57], v[56:57], s[16:17], v[28:29] op_sel_hi:[1,0,0]
v_pk_mul_f32 v[54:55], v[54:55], v[234:235]
v_pk_mul_f32 v[56:57], v[56:57], v[236:237]
v_exp_f32 v54, v54
buffer_load_dwordx4 v[84:87], v22, s[4:7], s82 offen sc1
)ASM"
R"ASM(
v_exp_f32 v55, v55
v_exp_f32 v56, v56
v_exp_f32 v57, v57
v_pk_add_f32 v[54:55], v[54:55], 1.0 op_sel_hi:[1,0]
v_pk_add_f32 v[56:57], v[56:57], 1.0 op_sel_hi:[1,0]
v_rcp_f32 v54, v54
buffer_load_dwordx4 v[88:91], v22, s[4:7], s77 offen sc1
v_rcp_f32 v55, v55
v_rcp_f32 v56, v56
v_rcp_f32 v57, v57
v_pk_mul_f32 v[234:235], v[234:235], v[54:55]
v_pk_mul_f32 v[236:237], v[236:237], v[56:57]
v_cvt_pk_bf16_f32 v114, v234, v235
v_cvt_pk_bf16_f32 v115, v236, v237
buffer_store_dwordx4 v[112:115], v0, s[4:7], s87 offen sc1
v_pk_fma_f32 v[210:211], v[30:31], v[26:27], v[210:211] op_sel_hi:[1,0,1]
v_pk_fma_f32 v[212:213], v[32:33], v[26:27], v[212:213] op_sel_hi:[1,0,1]
v_pk_fma_f32 v[210:211], v[210:211], v[26:27], v[42:43] op_sel:[0,1,0] op_sel_hi:[1,1,1]
v_pk_fma_f32 v[212:213], v[212:213], v[26:27], v[44:45] op_sel:[0,1,0] op_sel_hi:[1,1,1]
v_pk_mul_f32 v[54:55], v[210:211], v[210:211]
v_pk_mul_f32 v[56:57], v[212:213], v[212:213]
v_pk_fma_f32 v[54:55], v[54:55], s[16:17], v[28:29] op_sel_hi:[1,0,0]
v_pk_fma_f32 v[56:57], v[56:57], s[16:17], v[28:29] op_sel_hi:[1,0,0]
v_pk_mul_f32 v[54:55], v[54:55], v[210:211]
v_pk_mul_f32 v[56:57], v[56:57], v[212:213]
v_exp_f32 v54, v54
v_exp_f32 v55, v55
v_exp_f32 v56, v56
v_exp_f32 v57, v57
v_pk_add_f32 v[54:55], v[54:55], 1.0 op_sel_hi:[1,0]
v_pk_add_f32 v[56:57], v[56:57], 1.0 op_sel_hi:[1,0]
v_rcp_f32 v54, v54
v_rcp_f32 v55, v55
v_rcp_f32 v56, v56
v_rcp_f32 v57, v57
v_pk_mul_f32 v[210:211], v[210:211], v[54:55]
v_pk_mul_f32 v[212:213], v[212:213], v[56:57]
v_cvt_pk_bf16_f32 v116, v210, v211
v_cvt_pk_bf16_f32 v117, v212, v213
v_pk_fma_f32 v[250:251], v[34:35], v[26:27], v[250:251] op_sel_hi:[1,0,1]
v_pk_fma_f32 v[252:253], v[36:37], v[26:27], v[252:253] op_sel_hi:[1,0,1]
v_pk_fma_f32 v[250:251], v[250:251], v[26:27], v[46:47] op_sel:[0,1,0] op_sel_hi:[1,1,1]
v_pk_fma_f32 v[252:253], v[252:253], v[26:27], v[48:49] op_sel:[0,1,0] op_sel_hi:[1,1,1]
v_pk_mul_f32 v[54:55], v[250:251], v[250:251]
v_pk_mul_f32 v[56:57], v[252:253], v[252:253]
v_pk_fma_f32 v[54:55], v[54:55], s[16:17], v[28:29] op_sel_hi:[1,0,0]
v_pk_fma_f32 v[56:57], v[56:57], s[16:17], v[28:29] op_sel_hi:[1,0,0]
v_pk_mul_f32 v[54:55], v[54:55], v[250:251]
v_pk_mul_f32 v[56:57], v[56:57], v[252:253]
v_exp_f32 v54, v54
v_exp_f32 v55, v55
v_exp_f32 v56, v56
v_exp_f32 v57, v57
v_pk_add_f32 v[54:55], v[54:55], 1.0 op_sel_hi:[1,0]
v_pk_add_f32 v[56:57], v[56:57], 1.0 op_sel_hi:[1,0]
v_rcp_f32 v54, v54
v_rcp_f32 v55, v55
v_rcp_f32 v56, v56
v_rcp_f32 v57, v57
v_pk_mul_f32 v[250:251], v[250:251], v[54:55]
v_pk_mul_f32 v[252:253], v[252:253], v[56:57]
v_cvt_pk_bf16_f32 v118, v250, v251
v_cvt_pk_bf16_f32 v119, v252, v253
buffer_store_dwordx4 v[116:119], v0, s[4:7], s88 offen sc1
v_pk_fma_f32 v[202:203], v[38:39], v[24:25], v[202:203] op_sel_hi:[1,0,1]
s_mov_b32 s79, 12
s_mul_i32 s79, s79, 0x1400
s_add_u32 s79, s79, s63
buffer_load_dwordx4 a[240:243], v29, s[4:7], s79 offen sc1
v_pk_fma_f32 v[204:205], v[40:41], v[24:25], v[204:205] op_sel_hi:[1,0,1]
v_pk_fma_f32 v[202:203], v[202:203], v[24:25], v[50:51] op_sel:[0,1,0] op_sel_hi:[1,1,1]
v_pk_fma_f32 v[204:205], v[204:205], v[24:25], v[52:53] op_sel:[0,1,0] op_sel_hi:[1,1,1]
v_pk_mul_f32 v[54:55], v[202:203], v[202:203]
v_pk_mul_f32 v[56:57], v[204:205], v[204:205]
buffer_load_dwordx4 a[244:247], v29, s[4:7], s79 offen offset:1024 sc1
v_pk_fma_f32 v[54:55], v[54:55], s[16:17], v[28:29] op_sel_hi:[1,0,0]
v_pk_fma_f32 v[56:57], v[56:57], s[16:17], v[28:29] op_sel_hi:[1,0,0]
v_pk_mul_f32 v[54:55], v[54:55], v[202:203]
v_pk_mul_f32 v[56:57], v[56:57], v[204:205]
buffer_load_dwordx4 a[248:251], v29, s[4:7], s79 offen offset:2048 sc1
v_exp_f32 v54, v54
v_exp_f32 v55, v55
v_exp_f32 v56, v56
v_exp_f32 v57, v57
v_pk_add_f32 v[54:55], v[54:55], 1.0 op_sel_hi:[1,0]
buffer_load_dwordx4 a[252:255], v29, s[4:7], s79 offen offset:3072 sc1
v_pk_add_f32 v[56:57], v[56:57], 1.0 op_sel_hi:[1,0]
v_rcp_f32 v54, v54
v_rcp_f32 v55, v55
v_rcp_f32 v56, v56
s_add_u32 s79, s79, 0x1000
buffer_load_dwordx4 v[230:233], v29, s[4:7], s79 offen sc1
v_rcp_f32 v57, v57
v_pk_mul_f32 v[202:203], v[202:203], v[54:55]
v_pk_mul_f32 v[204:205], v[204:205], v[56:57]
v_cvt_pk_bf16_f32 v120, v202, v203
s_mov_b32 s79, 13
s_mul_i32 s79, s79, 0x1400
s_add_u32 s79, s79, s63
buffer_load_dwordx4 v[234:237], v29, s[4:7], s79 offen sc1
v_cvt_pk_bf16_f32 v121, v204, v205
buffer_store_dwordx2 v[120:121], v21, s[4:7], s89 offen sc1
v_pk_fma_f32 v[218:219], v[38:39], v[26:27], v[218:219] op_sel_hi:[1,0,1]
v_pk_fma_f32 v[220:221], v[40:41], v[26:27], v[220:221] op_sel_hi:[1,0,1]
v_pk_fma_f32 v[218:219], v[218:219], v[26:27], v[50:51] op_sel:[0,1,0] op_sel_hi:[1,1,1]
buffer_load_dwordx4 v[238:241], v29, s[4:7], s79 offen offset:1024 sc1
v_pk_fma_f32 v[220:221], v[220:221], v[26:27], v[52:53] op_sel:[0,1,0] op_sel_hi:[1,1,1]
v_pk_mul_f32 v[54:55], v[218:219], v[218:219]
v_pk_mul_f32 v[56:57], v[220:221], v[220:221]
v_pk_fma_f32 v[54:55], v[54:55], s[16:17], v[28:29] op_sel_hi:[1,0,0]
buffer_load_dwordx4 v[242:245], v29, s[4:7], s79 offen offset:2048 sc1
v_pk_fma_f32 v[56:57], v[56:57], s[16:17], v[28:29] op_sel_hi:[1,0,0]
v_pk_mul_f32 v[54:55], v[54:55], v[218:219]
v_pk_mul_f32 v[56:57], v[56:57], v[220:221]
v_exp_f32 v54, v54
v_exp_f32 v55, v55
buffer_load_dwordx4 v[246:249], v29, s[4:7], s79 offen offset:3072 sc1
v_exp_f32 v56, v56
v_exp_f32 v57, v57
v_pk_add_f32 v[54:55], v[54:55], 1.0 op_sel_hi:[1,0]
v_pk_add_f32 v[56:57], v[56:57], 1.0 op_sel_hi:[1,0]
s_add_u32 s79, s79, 0x1000
buffer_load_dwordx4 v[250:253], v29, s[4:7], s79 offen sc1
v_rcp_f32 v54, v54
v_rcp_f32 v55, v55
v_rcp_f32 v56, v56
v_rcp_f32 v57, v57
v_pk_mul_f32 v[218:219], v[218:219], v[54:55]
v_pk_mul_f32 v[220:221], v[220:221], v[56:57]
v_cvt_pk_bf16_f32 v122, v218, v219
v_cvt_pk_bf16_f32 v123, v220, v221
buffer_store_dwordx2 v[122:123], v21, s[4:7], s19 offen sc1
s_add_u32 s44, s84, 0x5000
s_waitcnt vmcnt(12)
s_barrier
buffer_store_dwordx2 v[4:5], v2, s[4:7], s44 offen sc1
v_mfma_f32_16x16x32_bf16 v[112:115], a[0:3], v[60:63], 0
s_add_u32 s43, s96, 2
v_mfma_f32_16x16x32_bf16 v[116:119], a[4:7], v[60:63], 0
s_and_b32 s43, s43, 7
v_mfma_f32_16x16x32_bf16 v[120:123], a[8:11], v[60:63], 0
v_lshl_add_u32 v20, s43, 10, v0
v_mfma_f32_16x16x32_bf16 v[124:127], a[12:15], v[60:63], 0
buffer_load_dwordx4 v[92:95], v20, s[4:7], s80 offen sc1
v_mfma_f32_16x16x32_bf16 v[128:131], a[16:19], v[60:63], 0
buffer_load_dwordx4 v[96:99], v20, s[4:7], s81 offen sc1
v_mfma_f32_16x16x32_bf16 v[132:135], a[0:3], v[64:67], 0
buffer_load_dwordx4 v[100:103], v20, s[4:7], s82 offen sc1
v_mfma_f32_16x16x32_bf16 v[136:139], a[4:7], v[64:67], 0
buffer_load_dwordx4 v[104:107], v20, s[4:7], s77 offen sc1
v_mfma_f32_16x16x32_bf16 v[140:143], a[8:11], v[64:67], 0
s_add_u32 s43, s96, 3
v_mfma_f32_16x16x32_bf16 v[144:147], a[12:15], v[64:67], 0
s_and_b32 s43, s43, 7
v_mfma_f32_16x16x32_bf16 v[148:151], a[16:19], v[64:67], 0
v_lshl_add_u32 v22, s43, 10, v0
v_mfma_f32_16x16x32_bf16 v[152:155], a[0:3], v[68:71], 0
buffer_load_dwordx4 v[192:195], v22, s[4:7], s80 offen sc1
v_mfma_f32_16x16x32_bf16 v[156:159], a[4:7], v[68:71], 0
buffer_load_dwordx4 v[196:199], v22, s[4:7], s81 offen sc1
v_mfma_f32_16x16x32_bf16 v[160:163], a[8:11], v[68:71], 0
buffer_load_dwordx4 v[200:203], v22, s[4:7], s82 offen sc1
v_mfma_f32_16x16x32_bf16 v[164:167], a[12:15], v[68:71], 0
buffer_load_dwordx4 v[204:207], v22, s[4:7], s77 offen sc1
v_mfma_f32_16x16x32_bf16 v[168:171], a[16:19], v[68:71], 0
v_mfma_f32_16x16x32_bf16 v[172:175], a[0:3], v[72:75], 0
v_mfma_f32_16x16x32_bf16 v[176:179], a[4:7], v[72:75], 0
v_mfma_f32_16x16x32_bf16 v[180:183], a[8:11], v[72:75], 0
v_mfma_f32_16x16x32_bf16 v[184:187], a[12:15], v[72:75], 0
v_mfma_f32_16x16x32_bf16 v[188:191], a[16:19], v[72:75], 0
v_mfma_f32_16x16x32_bf16 v[112:115], a[20:23], v[76:79], v[112:115]
s_add_u32 s43, s96, 4
v_mfma_f32_16x16x32_bf16 v[116:119], a[24:27], v[76:79], v[116:119]
v_mfma_f32_16x16x32_bf16 v[120:123], a[28:31], v[76:79], v[120:123]
s_and_b32 s43, s43, 7
v_mfma_f32_16x16x32_bf16 v[124:127], a[32:35], v[76:79], v[124:127]
v_mfma_f32_16x16x32_bf16 v[128:131], a[36:39], v[76:79], v[128:131]
v_lshl_add_u32 v20, s43, 10, v0
v_mfma_f32_16x16x32_bf16 v[132:135], a[20:23], v[80:83], v[132:135]
v_mfma_f32_16x16x32_bf16 v[136:139], a[24:27], v[80:83], v[136:139]
buffer_load_dwordx4 v[60:63], v20, s[4:7], s80 offen sc1
v_mfma_f32_16x16x32_bf16 v[140:143], a[28:31], v[80:83], v[140:143]
v_mfma_f32_16x16x32_bf16 v[144:147], a[32:35], v[80:83], v[144:147]
buffer_load_dwordx4 v[64:67], v20, s[4:7], s81 offen sc1
v_mfma_f32_16x16x32_bf16 v[148:151], a[36:39], v[80:83], v[148:151]
v_mfma_f32_16x16x32_bf16 v[152:155], a[20:23], v[84:87], v[152:155]
buffer_load_dwordx4 v[68:71], v20, s[4:7], s82 offen sc1
v_mfma_f32_16x16x32_bf16 v[156:159], a[24:27], v[84:87], v[156:159]
v_mfma_f32_16x16x32_bf16 v[160:163], a[28:31], v[84:87], v[160:163]
buffer_load_dwordx4 v[72:75], v20, s[4:7], s77 offen sc1
v_mfma_f32_16x16x32_bf16 v[164:167], a[32:35], v[84:87], v[164:167]
v_mfma_f32_16x16x32_bf16 v[168:171], a[36:39], v[84:87], v[168:171]
v_mfma_f32_16x16x32_bf16 v[172:175], a[20:23], v[88:91], v[172:175]
v_mfma_f32_16x16x32_bf16 v[176:179], a[24:27], v[88:91], v[176:179]
v_mfma_f32_16x16x32_bf16 v[180:183], a[28:31], v[88:91], v[180:183]
v_mfma_f32_16x16x32_bf16 v[184:187], a[32:35], v[88:91], v[184:187]
v_mfma_f32_16x16x32_bf16 v[188:191], a[36:39], v[88:91], v[188:191]
s_add_u32 s44, s84, 0x6000
s_waitcnt vmcnt(13)
s_barrier
)ASM"
R"ASM(
buffer_store_dwordx2 v[4:5], v2, s[4:7], s44 offen sc1
s_waitcnt vmcnt(12)
v_mfma_f32_16x16x32_bf16 v[112:115], a[40:43], v[92:95], v[112:115]
s_add_u32 s43, s96, 5
v_mfma_f32_16x16x32_bf16 v[116:119], a[44:47], v[92:95], v[116:119]
s_and_b32 s43, s43, 7
v_mfma_f32_16x16x32_bf16 v[120:123], a[48:51], v[92:95], v[120:123]
v_lshl_add_u32 v22, s43, 10, v0
v_mfma_f32_16x16x32_bf16 v[124:127], a[52:55], v[92:95], v[124:127]
buffer_load_dwordx4 v[76:79], v22, s[4:7], s80 offen sc1
v_mfma_f32_16x16x32_bf16 v[128:131], a[56:59], v[92:95], v[128:131]
buffer_load_dwordx4 v[80:83], v22, s[4:7], s81 offen sc1
s_waitcnt vmcnt(13)
v_mfma_f32_16x16x32_bf16 v[132:135], a[40:43], v[96:99], v[132:135]
buffer_load_dwordx4 v[84:87], v22, s[4:7], s82 offen sc1
v_mfma_f32_16x16x32_bf16 v[136:139], a[44:47], v[96:99], v[136:139]
buffer_load_dwordx4 v[88:91], v22, s[4:7], s77 offen sc1
v_mfma_f32_16x16x32_bf16 v[140:143], a[48:51], v[96:99], v[140:143]
s_add_u32 s45, s34, 1
v_mfma_f32_16x16x32_bf16 v[144:147], a[52:55], v[96:99], v[144:147]
s_and_b32 s45, s45, 3
v_mfma_f32_16x16x32_bf16 v[148:151], a[56:59], v[96:99], v[148:151]
s_mul_i32 s45, s45, 0x3c00
s_waitcnt vmcnt(14)
v_mfma_f32_16x16x32_bf16 v[152:155], a[40:43], v[100:103], v[152:155]
s_add_u32 s45, s45, 0
v_mfma_f32_16x16x32_bf16 v[156:159], a[44:47], v[100:103], v[156:159]
v_add_u32 v24, s45, v0
v_mfma_f32_16x16x32_bf16 v[160:163], a[48:51], v[100:103], v[160:163]
s_add_u32 s45, s34, 2
v_mfma_f32_16x16x32_bf16 v[164:167], a[52:55], v[100:103], v[164:167]
s_and_b32 s45, s45, 3
v_mfma_f32_16x16x32_bf16 v[168:171], a[56:59], v[100:103], v[168:171]
s_mul_i32 s45, s45, 0x3c00
s_waitcnt vmcnt(13)
v_mfma_f32_16x16x32_bf16 v[172:175], a[40:43], v[104:107], v[172:175]
s_add_u32 s45, s45, 0x1400
v_mfma_f32_16x16x32_bf16 v[176:179], a[44:47], v[104:107], v[176:179]
v_add_u32 v25, s45, v0
v_mfma_f32_16x16x32_bf16 v[180:183], a[48:51], v[104:107], v[180:183]
s_add_u32 s45, s34, 3
v_mfma_f32_16x16x32_bf16 v[184:187], a[52:55], v[104:107], v[184:187]
s_and_b32 s45, s45, 3
v_mfma_f32_16x16x32_bf16 v[188:191], a[56:59], v[104:107], v[188:191]
s_mul_i32 s45, s45, 0x3c00
s_waitcnt vmcnt(12)
v_mfma_f32_16x16x32_bf16 v[112:115], a[60:63], v[192:195], v[112:115]
s_mov_b32 s86, 0x5000
v_mfma_f32_16x16x32_bf16 v[116:119], a[64:67], v[192:195], v[116:119]
buffer_load_dwordx2 v[218:219], v3, s[4:7], s86 offen sc1
v_mfma_f32_16x16x32_bf16 v[120:123], a[68:71], v[192:195], v[120:123]
s_add_u32 s43, s96, 6
v_mfma_f32_16x16x32_bf16 v[124:127], a[72:75], v[192:195], v[124:127]
s_and_b32 s43, s43, 7
v_mfma_f32_16x16x32_bf16 v[128:131], a[76:79], v[192:195], v[128:131]
v_lshl_add_u32 v20, s43, 10, v0
s_waitcnt vmcnt(12)
v_mfma_f32_16x16x32_bf16 v[132:135], a[60:63], v[196:199], v[132:135]
buffer_load_dwordx4 v[92:95], v20, s[4:7], s80 offen sc1
v_mfma_f32_16x16x32_bf16 v[136:139], a[64:67], v[196:199], v[136:139]
buffer_load_dwordx4 v[96:99], v20, s[4:7], s81 offen sc1
v_mfma_f32_16x16x32_bf16 v[140:143], a[68:71], v[196:199], v[140:143]
buffer_load_dwordx4 v[100:103], v20, s[4:7], s82 offen sc1
v_mfma_f32_16x16x32_bf16 v[144:147], a[72:75], v[196:199], v[144:147]
buffer_load_dwordx4 v[104:107], v20, s[4:7], s77 offen sc1
v_mfma_f32_16x16x32_bf16 v[148:151], a[76:79], v[196:199], v[148:151]
s_add_u32 s45, s45, 0x2800
s_waitcnt vmcnt(15)
v_mfma_f32_16x16x32_bf16 v[152:155], a[60:63], v[200:203], v[152:155]
v_add_u32 v26, s45, v0
v_mfma_f32_16x16x32_bf16 v[156:159], a[64:67], v[200:203], v[156:159]
s_mul_i32 s45, s34, 0x3c00
v_mfma_f32_16x16x32_bf16 v[160:163], a[68:71], v[200:203], v[160:163]
v_add_u32 v27, s45, v0
v_mfma_f32_16x16x32_bf16 v[164:167], a[72:75], v[200:203], v[164:167]
s_mov_b32 s12, s28
v_mfma_f32_16x16x32_bf16 v[168:171], a[76:79], v[200:203], v[168:171]
s_and_b32 s13, s29, 0xffff
s_waitcnt vmcnt(14)
v_mfma_f32_16x16x32_bf16 v[172:175], a[60:63], v[204:207], v[172:175]
s_mov_b32 s14, 0x500
v_mfma_f32_16x16x32_bf16 v[176:179], a[64:67], v[204:207], v[176:179]
s_mov_b32 s15, 0x20000
v_mfma_f32_16x16x32_bf16 v[180:183], a[68:71], v[204:207], v[180:183]
v_lshrrev_b32 v28, 8, v0
v_mfma_f32_16x16x32_bf16 v[184:187], a[72:75], v[204:207], v[184:187]
v_lshlrev_b32 v28, 3, v28
v_mfma_f32_16x16x32_bf16 v[188:191], a[76:79], v[204:207], v[188:191]
buffer_load_dwordx2 v[208:209], v28, s[12:15], s50 offen
s_waitcnt vmcnt(14)
v_mfma_f32_16x16x32_bf16 v[112:115], a[80:83], v[60:63], v[112:115]
s_add_u32 s43, s96, 7
v_mfma_f32_16x16x32_bf16 v[116:119], a[84:87], v[60:63], v[116:119]
s_and_b32 s43, s43, 7
v_mfma_f32_16x16x32_bf16 v[120:123], a[88:91], v[60:63], v[120:123]
v_lshl_add_u32 v22, s43, 10, v0
v_mfma_f32_16x16x32_bf16 v[124:127], a[92:95], v[60:63], v[124:127]
buffer_load_dwordx4 v[192:195], v22, s[4:7], s80 offen sc1
v_mfma_f32_16x16x32_bf16 v[128:131], a[96:99], v[60:63], v[128:131]
buffer_load_dwordx4 v[196:199], v22, s[4:7], s81 offen sc1
s_waitcnt vmcnt(15)
v_mfma_f32_16x16x32_bf16 v[132:135], a[80:83], v[64:67], v[132:135]
buffer_load_dwordx4 v[200:203], v22, s[4:7], s82 offen sc1
v_mfma_f32_16x16x32_bf16 v[136:139], a[84:87], v[64:67], v[136:139]
buffer_load_dwordx4 v[204:207], v22, s[4:7], s77 offen sc1
v_mfma_f32_16x16x32_bf16 v[140:143], a[88:91], v[64:67], v[140:143]
buffer_load_dwordx2 v[210:211], v28, s[12:15], s50 offen offset:32
v_mfma_f32_16x16x32_bf16 v[144:147], a[92:95], v[64:67], v[144:147]
buffer_load_dwordx2 v[212:213], v28, s[12:15], s50 offen offset:64
v_mfma_f32_16x16x32_bf16 v[148:151], a[96:99], v[64:67], v[148:151]
buffer_load_dwordx2 v[214:215], v28, s[12:15], s50 offen offset:96
s_waitcnt vmcnt(19)
v_mfma_f32_16x16x32_bf16 v[152:155], a[80:83], v[68:71], v[152:155]
buffer_load_dwordx2 v[216:217], v28, s[12:15], s50 offen offset:128
v_mfma_f32_16x16x32_bf16 v[156:159], a[84:87], v[68:71], v[156:159]
v_mfma_f32_16x16x32_bf16 v[160:163], a[88:91], v[68:71], v[160:163]
v_mfma_f32_16x16x32_bf16 v[164:167], a[92:95], v[68:71], v[164:167]
v_mfma_f32_16x16x32_bf16 v[168:171], a[96:99], v[68:71], v[168:171]
s_waitcnt vmcnt(19)
v_mfma_f32_16x16x32_bf16 v[172:175], a[80:83], v[72:75], v[172:175]
v_mfma_f32_16x16x32_bf16 v[176:179], a[84:87], v[72:75], v[176:179]
v_mfma_f32_16x16x32_bf16 v[180:183], a[88:91], v[72:75], v[180:183]
v_mfma_f32_16x16x32_bf16 v[184:187], a[92:95], v[72:75], v[184:187]
v_mfma_f32_16x16x32_bf16 v[188:191], a[96:99], v[72:75], v[188:191]
s_waitcnt vmcnt(13)
v_cmp_eq_u64_e64 s[94:95], s[40:41], v[218:219]
s_nop 0
s_cmp_eq_u64 s[94:95], exec
s_cbranch_scc0 .Lk_g2s_16
.Lk_g2b_17:
v_mfma_f32_16x16x32_bf16 v[112:115], a[100:103], v[76:79], v[112:115]
s_add_u32 s43, s96, 8
v_mfma_f32_16x16x32_bf16 v[116:119], a[104:107], v[76:79], v[116:119]
v_mfma_f32_16x16x32_bf16 v[120:123], a[108:111], v[76:79], v[120:123]
s_and_b32 s43, s43, 3
v_mfma_f32_16x16x32_bf16 v[124:127], a[112:115], v[76:79], v[124:127]
v_mfma_f32_16x16x32_bf16 v[128:131], a[116:119], v[76:79], v[128:131]
s_or_b32 s43, s43, 8
v_mfma_f32_16x16x32_bf16 v[132:135], a[100:103], v[80:83], v[132:135]
v_mfma_f32_16x16x32_bf16 v[136:139], a[104:107], v[80:83], v[136:139]
v_lshl_add_u32 v20, s43, 10, v0
v_mfma_f32_16x16x32_bf16 v[140:143], a[108:111], v[80:83], v[140:143]
v_mfma_f32_16x16x32_bf16 v[144:147], a[112:115], v[80:83], v[144:147]
buffer_load_dwordx4 v[60:63], v20, s[4:7], s80 offen sc1
v_mfma_f32_16x16x32_bf16 v[148:151], a[116:119], v[80:83], v[148:151]
v_mfma_f32_16x16x32_bf16 v[152:155], a[100:103], v[84:87], v[152:155]
buffer_load_dwordx4 v[64:67], v20, s[4:7], s81 offen sc1
v_mfma_f32_16x16x32_bf16 v[156:159], a[104:107], v[84:87], v[156:159]
v_mfma_f32_16x16x32_bf16 v[160:163], a[108:111], v[84:87], v[160:163]
buffer_load_dwordx4 v[68:71], v20, s[4:7], s82 offen sc1
v_mfma_f32_16x16x32_bf16 v[164:167], a[112:115], v[84:87], v[164:167]
v_mfma_f32_16x16x32_bf16 v[168:171], a[116:119], v[84:87], v[168:171]
buffer_load_dwordx4 v[72:75], v20, s[4:7], s77 offen sc1
v_mfma_f32_16x16x32_bf16 v[172:175], a[100:103], v[88:91], v[172:175]
v_mfma_f32_16x16x32_bf16 v[176:179], a[104:107], v[88:91], v[176:179]
v_mfma_f32_16x16x32_bf16 v[180:183], a[108:111], v[88:91], v[180:183]
v_mfma_f32_16x16x32_bf16 v[184:187], a[112:115], v[88:91], v[184:187]
v_mfma_f32_16x16x32_bf16 v[188:191], a[116:119], v[88:91], v[188:191]
s_waitcnt vmcnt(16)
v_mfma_f32_16x16x32_bf16 v[112:115], a[120:123], v[92:95], v[112:115]
s_mov_b32 s86, 0x6000
v_mfma_f32_16x16x32_bf16 v[116:119], a[124:127], v[92:95], v[116:119]
buffer_load_dwordx2 v[218:219], v3, s[4:7], s86 offen sc1
v_mfma_f32_16x16x32_bf16 v[120:123], a[128:131], v[92:95], v[120:123]
s_add_u32 s43, s96, 9
v_mfma_f32_16x16x32_bf16 v[124:127], a[132:135], v[92:95], v[124:127]
s_and_b32 s43, s43, 3
v_mfma_f32_16x16x32_bf16 v[128:131], a[136:139], v[92:95], v[128:131]
s_or_b32 s43, s43, 8
s_waitcnt vmcnt(16)
v_mfma_f32_16x16x32_bf16 v[132:135], a[120:123], v[96:99], v[132:135]
v_lshlrev_b32 v220, 16, v10
v_and_b32 v221, s59, v10
v_mfma_f32_16x16x32_bf16 v[136:139], a[124:127], v[96:99], v[136:139]
v_lshlrev_b32 v222, 16, v11
v_and_b32 v223, s59, v11
v_mfma_f32_16x16x32_bf16 v[140:143], a[128:131], v[96:99], v[140:143]
v_add_f32 v172, v172, v220
v_add_f32 v173, v173, v221
v_mfma_f32_16x16x32_bf16 v[144:147], a[132:135], v[96:99], v[144:147]
v_add_f32 v174, v174, v222
v_add_f32 v175, v175, v223
v_mfma_f32_16x16x32_bf16 v[148:151], a[136:139], v[96:99], v[148:151]
s_waitcnt vmcnt(13)
v_lshlrev_b32 v220, 16, v208
v_and_b32 v221, s59, v208
v_mfma_f32_16x16x32_bf16 v[152:155], a[120:123], v[100:103], v[152:155]
v_lshlrev_b32 v222, 16, v209
v_and_b32 v223, s59, v209
v_mfma_f32_16x16x32_bf16 v[156:159], a[124:127], v[100:103], v[156:159]
v_add_f32 v172, v172, v220
v_add_f32 v173, v173, v221
v_mfma_f32_16x16x32_bf16 v[160:163], a[128:131], v[100:103], v[160:163]
v_add_f32 v174, v174, v222
v_add_f32 v175, v175, v223
v_mfma_f32_16x16x32_bf16 v[164:167], a[132:135], v[100:103], v[164:167]
v_lshl_add_u32 v22, s43, 10, v0
v_mfma_f32_16x16x32_bf16 v[168:171], a[136:139], v[100:103], v[168:171]
buffer_load_dwordx4 v[76:79], v22, s[4:7], s80 offen sc1
v_mfma_f32_16x16x32_bf16 v[172:175], a[120:123], v[104:107], v[172:175]
)ASM"
R"ASM(
buffer_load_dwordx4 v[80:83], v22, s[4:7], s81 offen sc1
v_mfma_f32_16x16x32_bf16 v[176:179], a[124:127], v[104:107], v[176:179]
buffer_load_dwordx4 v[84:87], v22, s[4:7], s82 offen sc1
v_mfma_f32_16x16x32_bf16 v[180:183], a[128:131], v[104:107], v[180:183]
buffer_load_dwordx4 v[88:91], v22, s[4:7], s77 offen sc1
v_mfma_f32_16x16x32_bf16 v[184:187], a[132:135], v[104:107], v[184:187]
v_mfma_f32_16x16x32_bf16 v[188:191], a[136:139], v[104:107], v[188:191]
s_waitcnt vmcnt(16)
v_mfma_f32_16x16x32_bf16 v[112:115], a[140:143], v[192:195], v[112:115]
s_add_u32 s43, s96, 10
v_mfma_f32_16x16x32_bf16 v[116:119], a[144:147], v[192:195], v[116:119]
s_and_b32 s43, s43, 3
v_mfma_f32_16x16x32_bf16 v[120:123], a[148:151], v[192:195], v[120:123]
s_or_b32 s43, s43, 8
v_mfma_f32_16x16x32_bf16 v[124:127], a[152:155], v[192:195], v[124:127]
v_lshl_add_u32 v20, s43, 10, v0
v_mfma_f32_16x16x32_bf16 v[128:131], a[156:159], v[192:195], v[128:131]
buffer_load_dwordx4 v[92:95], v20, s[4:7], s80 offen sc1
s_waitcnt vmcnt(16)
v_mfma_f32_16x16x32_bf16 v[132:135], a[140:143], v[196:199], v[132:135]
v_lshlrev_b32 v220, 16, v12
v_and_b32 v221, s59, v12
v_mfma_f32_16x16x32_bf16 v[136:139], a[144:147], v[196:199], v[136:139]
v_lshlrev_b32 v222, 16, v13
v_and_b32 v223, s59, v13
v_mfma_f32_16x16x32_bf16 v[140:143], a[148:151], v[196:199], v[140:143]
v_add_f32 v176, v176, v220
v_add_f32 v177, v177, v221
v_mfma_f32_16x16x32_bf16 v[144:147], a[152:155], v[196:199], v[144:147]
v_add_f32 v178, v178, v222
v_add_f32 v179, v179, v223
v_mfma_f32_16x16x32_bf16 v[148:151], a[156:159], v[196:199], v[148:151]
s_waitcnt vmcnt(13)
v_lshlrev_b32 v220, 16, v210
v_and_b32 v221, s59, v210
v_mfma_f32_16x16x32_bf16 v[152:155], a[140:143], v[200:203], v[152:155]
v_lshlrev_b32 v222, 16, v211
v_and_b32 v223, s59, v211
v_mfma_f32_16x16x32_bf16 v[156:159], a[144:147], v[200:203], v[156:159]
v_add_f32 v176, v176, v220
v_add_f32 v177, v177, v221
v_mfma_f32_16x16x32_bf16 v[160:163], a[148:151], v[200:203], v[160:163]
v_add_f32 v178, v178, v222
v_add_f32 v179, v179, v223
v_mfma_f32_16x16x32_bf16 v[164:167], a[152:155], v[200:203], v[164:167]
buffer_load_dwordx4 v[96:99], v20, s[4:7], s81 offen sc1
v_mfma_f32_16x16x32_bf16 v[168:171], a[156:159], v[200:203], v[168:171]
buffer_load_dwordx4 v[100:103], v20, s[4:7], s82 offen sc1
v_mfma_f32_16x16x32_bf16 v[172:175], a[140:143], v[204:207], v[172:175]
buffer_load_dwordx4 v[104:107], v20, s[4:7], s77 offen sc1
v_mfma_f32_16x16x32_bf16 v[176:179], a[144:147], v[204:207], v[176:179]
v_mfma_f32_16x16x32_bf16 v[180:183], a[148:151], v[204:207], v[180:183]
v_mfma_f32_16x16x32_bf16 v[184:187], a[152:155], v[204:207], v[184:187]
v_mfma_f32_16x16x32_bf16 v[188:191], a[156:159], v[204:207], v[188:191]
s_waitcnt vmcnt(8)
v_cmp_eq_u64_e64 s[94:95], s[40:41], v[218:219]
s_nop 0
s_cmp_eq_u64 s[94:95], exec
s_cbranch_scc0 .Lk_g3s_19
.Lk_g3b_20:
v_mfma_f32_16x16x32_bf16 v[112:115], a[160:163], v[60:63], v[112:115]
s_add_u32 s43, s96, 11
v_mfma_f32_16x16x32_bf16 v[116:119], a[164:167], v[60:63], v[116:119]
s_and_b32 s43, s43, 3
v_mfma_f32_16x16x32_bf16 v[120:123], a[168:171], v[60:63], v[120:123]
s_or_b32 s43, s43, 8
v_mfma_f32_16x16x32_bf16 v[124:127], a[172:175], v[60:63], v[124:127]
v_lshl_add_u32 v22, s43, 10, v0
v_mfma_f32_16x16x32_bf16 v[128:131], a[176:179], v[60:63], v[128:131]
buffer_load_dwordx4 v[192:195], v22, s[4:7], s80 offen sc1
v_mfma_f32_16x16x32_bf16 v[132:135], a[160:163], v[64:67], v[132:135]
v_lshlrev_b32 v220, 16, v14
v_and_b32 v221, s59, v14
v_mfma_f32_16x16x32_bf16 v[136:139], a[164:167], v[64:67], v[136:139]
v_lshlrev_b32 v222, 16, v15
v_and_b32 v223, s59, v15
v_mfma_f32_16x16x32_bf16 v[140:143], a[168:171], v[64:67], v[140:143]
v_add_f32 v180, v180, v220
v_add_f32 v181, v181, v221
v_mfma_f32_16x16x32_bf16 v[144:147], a[172:175], v[64:67], v[144:147]
v_add_f32 v182, v182, v222
v_add_f32 v183, v183, v223
v_mfma_f32_16x16x32_bf16 v[148:151], a[176:179], v[64:67], v[148:151]
v_lshlrev_b32 v220, 16, v212
v_and_b32 v221, s59, v212
v_mfma_f32_16x16x32_bf16 v[152:155], a[160:163], v[68:71], v[152:155]
v_lshlrev_b32 v222, 16, v213
v_and_b32 v223, s59, v213
v_mfma_f32_16x16x32_bf16 v[156:159], a[164:167], v[68:71], v[156:159]
v_add_f32 v180, v180, v220
v_add_f32 v181, v181, v221
v_mfma_f32_16x16x32_bf16 v[160:163], a[168:171], v[68:71], v[160:163]
v_add_f32 v182, v182, v222
v_add_f32 v183, v183, v223
v_mfma_f32_16x16x32_bf16 v[164:167], a[172:175], v[68:71], v[164:167]
buffer_load_dwordx4 v[196:199], v22, s[4:7], s81 offen sc1
v_mfma_f32_16x16x32_bf16 v[168:171], a[176:179], v[68:71], v[168:171]
buffer_load_dwordx4 v[200:203], v22, s[4:7], s82 offen sc1
v_mfma_f32_16x16x32_bf16 v[172:175], a[160:163], v[72:75], v[172:175]
buffer_load_dwordx4 v[204:207], v22, s[4:7], s77 offen sc1
v_mfma_f32_16x16x32_bf16 v[176:179], a[164:167], v[72:75], v[176:179]
v_mfma_f32_16x16x32_bf16 v[180:183], a[168:171], v[72:75], v[180:183]
v_mfma_f32_16x16x32_bf16 v[184:187], a[172:175], v[72:75], v[184:187]
v_mfma_f32_16x16x32_bf16 v[188:191], a[176:179], v[72:75], v[188:191]
s_waitcnt vmcnt(11)
v_mfma_f32_16x16x32_bf16 v[112:115], a[180:183], v[76:79], v[112:115]
s_or_b32 s43, s83, 0x3000
v_mfma_f32_16x16x32_bf16 v[116:119], a[184:187], v[76:79], v[116:119]
v_or_b32 v20, s43, v1
v_mfma_f32_16x16x32_bf16 v[120:123], a[188:191], v[76:79], v[120:123]
buffer_load_dwordx2 v[60:61], v20, s[4:7], s80 offen sc1
v_mfma_f32_16x16x32_bf16 v[124:127], a[192:195], v[76:79], v[124:127]
buffer_load_dwordx2 v[62:63], v20, s[4:7], s80 offen offset:512 sc1
v_mfma_f32_16x16x32_bf16 v[128:131], a[196:199], v[76:79], v[128:131]
buffer_load_dwordx2 v[64:65], v20, s[4:7], s81 offen sc1
s_waitcnt vmcnt(13)
v_mfma_f32_16x16x32_bf16 v[132:135], a[180:183], v[80:83], v[132:135]
v_lshlrev_b32 v220, 16, v16
v_and_b32 v221, s59, v16
v_mfma_f32_16x16x32_bf16 v[136:139], a[184:187], v[80:83], v[136:139]
v_lshlrev_b32 v222, 16, v17
v_and_b32 v223, s59, v17
v_mfma_f32_16x16x32_bf16 v[140:143], a[188:191], v[80:83], v[140:143]
v_add_f32 v184, v184, v220
v_add_f32 v185, v185, v221
v_mfma_f32_16x16x32_bf16 v[144:147], a[192:195], v[80:83], v[144:147]
v_add_f32 v186, v186, v222
v_add_f32 v187, v187, v223
v_mfma_f32_16x16x32_bf16 v[148:151], a[196:199], v[80:83], v[148:151]
v_lshlrev_b32 v220, 16, v214
v_and_b32 v221, s59, v214
s_waitcnt vmcnt(12)
v_mfma_f32_16x16x32_bf16 v[152:155], a[180:183], v[84:87], v[152:155]
v_lshlrev_b32 v222, 16, v215
v_and_b32 v223, s59, v215
v_mfma_f32_16x16x32_bf16 v[156:159], a[184:187], v[84:87], v[156:159]
v_add_f32 v184, v184, v220
v_add_f32 v185, v185, v221
v_mfma_f32_16x16x32_bf16 v[160:163], a[188:191], v[84:87], v[160:163]
v_add_f32 v186, v186, v222
v_add_f32 v187, v187, v223
v_mfma_f32_16x16x32_bf16 v[164:167], a[192:195], v[84:87], v[164:167]
buffer_load_dwordx2 v[66:67], v20, s[4:7], s81 offen offset:512 sc1
v_mfma_f32_16x16x32_bf16 v[168:171], a[196:199], v[84:87], v[168:171]
buffer_load_dwordx2 v[68:69], v20, s[4:7], s82 offen sc1
s_waitcnt vmcnt(13)
v_mfma_f32_16x16x32_bf16 v[172:175], a[180:183], v[88:91], v[172:175]
buffer_load_dwordx2 v[70:71], v20, s[4:7], s82 offen offset:512 sc1
v_mfma_f32_16x16x32_bf16 v[176:179], a[184:187], v[88:91], v[176:179]
buffer_load_dwordx2 v[72:73], v20, s[4:7], s77 offen sc1
v_mfma_f32_16x16x32_bf16 v[180:183], a[188:191], v[88:91], v[180:183]
buffer_load_dwordx2 v[74:75], v20, s[4:7], s77 offen offset:512 sc1
v_mfma_f32_16x16x32_bf16 v[184:187], a[192:195], v[88:91], v[184:187]
v_mfma_f32_16x16x32_bf16 v[188:191], a[196:199], v[88:91], v[188:191]
s_waitcnt vmcnt(15)
v_mfma_f32_16x16x32_bf16 v[112:115], a[200:203], v[92:95], v[112:115]
s_or_b32 s43, s83, 0x3400
v_mfma_f32_16x16x32_bf16 v[116:119], a[204:207], v[92:95], v[116:119]
v_or_b32 v22, s43, v1
v_mfma_f32_16x16x32_bf16 v[120:123], a[208:211], v[92:95], v[120:123]
buffer_load_dwordx2 v[76:77], v22, s[4:7], s80 offen sc1
v_mfma_f32_16x16x32_bf16 v[124:127], a[212:215], v[92:95], v[124:127]
buffer_load_dwordx2 v[78:79], v22, s[4:7], s80 offen offset:512 sc1
v_mfma_f32_16x16x32_bf16 v[128:131], a[216:219], v[92:95], v[128:131]
buffer_load_dwordx2 v[80:81], v22, s[4:7], s81 offen sc1
s_waitcnt vmcnt(17)
v_mfma_f32_16x16x32_bf16 v[132:135], a[200:203], v[96:99], v[132:135]
v_lshlrev_b32 v220, 16, v18
v_and_b32 v221, s59, v18
v_mfma_f32_16x16x32_bf16 v[136:139], a[204:207], v[96:99], v[136:139]
v_lshlrev_b32 v222, 16, v19
v_and_b32 v223, s59, v19
v_mfma_f32_16x16x32_bf16 v[140:143], a[208:211], v[96:99], v[140:143]
v_add_f32 v188, v188, v220
v_add_f32 v189, v189, v221
v_mfma_f32_16x16x32_bf16 v[144:147], a[212:215], v[96:99], v[144:147]
v_add_f32 v190, v190, v222
v_add_f32 v191, v191, v223
v_mfma_f32_16x16x32_bf16 v[148:151], a[216:219], v[96:99], v[148:151]
v_lshlrev_b32 v220, 16, v216
v_and_b32 v221, s59, v216
s_waitcnt vmcnt(16)
v_mfma_f32_16x16x32_bf16 v[152:155], a[200:203], v[100:103], v[152:155]
v_lshlrev_b32 v222, 16, v217
v_and_b32 v223, s59, v217
v_mfma_f32_16x16x32_bf16 v[156:159], a[204:207], v[100:103], v[156:159]
v_add_f32 v188, v188, v220
v_add_f32 v189, v189, v221
v_mfma_f32_16x16x32_bf16 v[160:163], a[208:211], v[100:103], v[160:163]
v_add_f32 v190, v190, v222
v_add_f32 v191, v191, v223
v_mfma_f32_16x16x32_bf16 v[164:167], a[212:215], v[100:103], v[164:167]
buffer_load_dwordx2 v[82:83], v22, s[4:7], s81 offen offset:512 sc1
v_mfma_f32_16x16x32_bf16 v[168:171], a[216:219], v[100:103], v[168:171]
buffer_load_dwordx2 v[84:85], v22, s[4:7], s82 offen sc1
s_waitcnt vmcnt(17)
v_mfma_f32_16x16x32_bf16 v[172:175], a[200:203], v[104:107], v[172:175]
buffer_load_dwordx2 v[86:87], v22, s[4:7], s82 offen offset:512 sc1
v_mfma_f32_16x16x32_bf16 v[176:179], a[204:207], v[104:107], v[176:179]
buffer_load_dwordx2 v[88:89], v22, s[4:7], s77 offen sc1
)ASM"
R"ASM(
v_mfma_f32_16x16x32_bf16 v[180:183], a[208:211], v[104:107], v[180:183]
buffer_load_dwordx2 v[90:91], v22, s[4:7], s77 offen offset:512 sc1
v_mfma_f32_16x16x32_bf16 v[184:187], a[212:215], v[104:107], v[184:187]
v_mfma_f32_16x16x32_bf16 v[188:191], a[216:219], v[104:107], v[188:191]
s_waitcnt vmcnt(19)
v_mfma_f32_16x16x32_bf16 v[112:115], a[220:223], v[192:195], v[112:115]
v_mfma_f32_16x16x32_bf16 v[116:119], a[224:227], v[192:195], v[116:119]
v_mfma_f32_16x16x32_bf16 v[120:123], a[228:231], v[192:195], v[120:123]
v_mfma_f32_16x16x32_bf16 v[124:127], a[232:235], v[192:195], v[124:127]
v_mfma_f32_16x16x32_bf16 v[128:131], a[236:239], v[192:195], v[128:131]
s_waitcnt vmcnt(14)
v_mfma_f32_16x16x32_bf16 v[112:115], a[240:243], v[60:63], v[112:115]
v_mfma_f32_16x16x32_bf16 v[116:119], a[244:247], v[60:63], v[116:119]
v_mfma_f32_16x16x32_bf16 v[120:123], a[248:251], v[60:63], v[120:123]
v_mfma_f32_16x16x32_bf16 v[124:127], a[252:255], v[60:63], v[124:127]
v_mfma_f32_16x16x32_bf16 v[128:131], v[230:233], v[60:63], v[128:131]
s_waitcnt vmcnt(6)
v_mfma_f32_16x16x32_bf16 v[112:115], v[234:237], v[76:79], v[112:115]
v_mfma_f32_16x16x32_bf16 v[116:119], v[238:241], v[76:79], v[116:119]
v_mfma_f32_16x16x32_bf16 v[120:123], v[242:245], v[76:79], v[120:123]
v_mfma_f32_16x16x32_bf16 v[124:127], v[246:249], v[76:79], v[124:127]
v_mfma_f32_16x16x32_bf16 v[128:131], v[250:253], v[76:79], v[128:131]
v_mfma_f32_16x16x32_bf16 v[132:135], a[220:223], v[196:199], v[132:135]
v_mfma_f32_16x16x32_bf16 v[136:139], a[224:227], v[196:199], v[136:139]
v_mfma_f32_16x16x32_bf16 v[140:143], a[228:231], v[196:199], v[140:143]
s_nop 0
ds_write_b128 v24, v[112:115]
v_mfma_f32_16x16x32_bf16 v[144:147], a[232:235], v[196:199], v[144:147]
v_mfma_f32_16x16x32_bf16 v[148:151], a[236:239], v[196:199], v[148:151]
ds_write_b128 v24, v[116:119] offset:1024
v_mfma_f32_16x16x32_bf16 v[132:135], a[240:243], v[64:67], v[132:135]
v_mfma_f32_16x16x32_bf16 v[136:139], a[244:247], v[64:67], v[136:139]
ds_write_b128 v24, v[120:123] offset:2048
v_mfma_f32_16x16x32_bf16 v[140:143], a[248:251], v[64:67], v[140:143]
v_mfma_f32_16x16x32_bf16 v[144:147], a[252:255], v[64:67], v[144:147]
ds_write_b128 v24, v[124:127] offset:3072
v_mfma_f32_16x16x32_bf16 v[148:151], v[230:233], v[64:67], v[148:151]
s_waitcnt vmcnt(4)
v_mfma_f32_16x16x32_bf16 v[132:135], v[234:237], v[80:83], v[132:135]
ds_write_b128 v24, v[128:131] offset:4096
v_mfma_f32_16x16x32_bf16 v[136:139], v[238:241], v[80:83], v[136:139]
v_mfma_f32_16x16x32_bf16 v[140:143], v[242:245], v[80:83], v[140:143]
v_mfma_f32_16x16x32_bf16 v[144:147], v[246:249], v[80:83], v[144:147]
v_mfma_f32_16x16x32_bf16 v[148:151], v[250:253], v[80:83], v[148:151]
v_mfma_f32_16x16x32_bf16 v[152:155], a[220:223], v[200:203], v[152:155]
v_mfma_f32_16x16x32_bf16 v[156:159], a[224:227], v[200:203], v[156:159]
v_mfma_f32_16x16x32_bf16 v[160:163], a[228:231], v[200:203], v[160:163]
ds_write_b128 v25, v[132:135]
v_mfma_f32_16x16x32_bf16 v[164:167], a[232:235], v[200:203], v[164:167]
v_mfma_f32_16x16x32_bf16 v[168:171], a[236:239], v[200:203], v[168:171]
ds_write_b128 v25, v[136:139] offset:1024
v_mfma_f32_16x16x32_bf16 v[152:155], a[240:243], v[68:71], v[152:155]
v_mfma_f32_16x16x32_bf16 v[156:159], a[244:247], v[68:71], v[156:159]
ds_write_b128 v25, v[140:143] offset:2048
v_mfma_f32_16x16x32_bf16 v[160:163], a[248:251], v[68:71], v[160:163]
v_mfma_f32_16x16x32_bf16 v[164:167], a[252:255], v[68:71], v[164:167]
ds_write_b128 v25, v[144:147] offset:3072
v_mfma_f32_16x16x32_bf16 v[168:171], v[230:233], v[68:71], v[168:171]
s_waitcnt vmcnt(2)
v_mfma_f32_16x16x32_bf16 v[152:155], v[234:237], v[84:87], v[152:155]
ds_write_b128 v25, v[148:151] offset:4096
v_mfma_f32_16x16x32_bf16 v[156:159], v[238:241], v[84:87], v[156:159]
v_mfma_f32_16x16x32_bf16 v[160:163], v[242:245], v[84:87], v[160:163]
v_mfma_f32_16x16x32_bf16 v[164:167], v[246:249], v[84:87], v[164:167]
v_mfma_f32_16x16x32_bf16 v[168:171], v[250:253], v[84:87], v[168:171]
v_mfma_f32_16x16x32_bf16 v[172:175], a[220:223], v[204:207], v[172:175]
v_mfma_f32_16x16x32_bf16 v[176:179], a[224:227], v[204:207], v[176:179]
v_mfma_f32_16x16x32_bf16 v[180:183], a[228:231], v[204:207], v[180:183]
ds_write_b128 v26, v[152:155]
v_mfma_f32_16x16x32_bf16 v[184:187], a[232:235], v[204:207], v[184:187]
v_mfma_f32_16x16x32_bf16 v[188:191], a[236:239], v[204:207], v[188:191]
ds_write_b128 v26, v[156:159] offset:1024
v_mfma_f32_16x16x32_bf16 v[172:175], a[240:243], v[72:75], v[172:175]
v_mfma_f32_16x16x32_bf16 v[176:179], a[244:247], v[72:75], v[176:179]
ds_write_b128 v26, v[160:163] offset:2048
v_mfma_f32_16x16x32_bf16 v[180:183], a[248:251], v[72:75], v[180:183]
v_mfma_f32_16x16x32_bf16 v[184:187], a[252:255], v[72:75], v[184:187]
ds_write_b128 v26, v[164:167] offset:3072
v_mfma_f32_16x16x32_bf16 v[188:191], v[230:233], v[72:75], v[188:191]
s_waitcnt vmcnt(0)
v_mfma_f32_16x16x32_bf16 v[172:175], v[234:237], v[88:91], v[172:175]
ds_write_b128 v26, v[168:171] offset:4096
v_mfma_f32_16x16x32_bf16 v[176:179], v[238:241], v[88:91], v[176:179]
v_mfma_f32_16x16x32_bf16 v[180:183], v[242:245], v[88:91], v[180:183]
v_mfma_f32_16x16x32_bf16 v[184:187], v[246:249], v[88:91], v[184:187]
v_mfma_f32_16x16x32_bf16 v[188:191], v[250:253], v[88:91], v[188:191]
s_mov_b32 s12, s30
s_and_b32 s13, s31, 0xffff
s_mov_b32 s14, 0x280000
s_mov_b32 s15, 0x20000
s_waitcnt lgkmcnt(0)
s_barrier
ds_read_b128 v[32:35], v27
ds_read_b128 v[52:55], v27 offset:5120
ds_read_b128 v[72:75], v27 offset:10240
ds_read_b128 v[36:39], v27 offset:1024
ds_read_b128 v[56:59], v27 offset:6144
ds_read_b128 v[76:79], v27 offset:11264
ds_read_b128 v[40:43], v27 offset:2048
ds_read_b128 v[60:63], v27 offset:7168
ds_read_b128 v[80:83], v27 offset:12288
ds_read_b128 v[44:47], v27 offset:3072
ds_read_b128 v[64:67], v27 offset:8192
ds_read_b128 v[84:87], v27 offset:13312
ds_read_b128 v[48:51], v27 offset:4096
ds_read_b128 v[68:71], v27 offset:9216
ds_read_b128 v[88:91], v27 offset:14336
v_lshrrev_b32 v25, 4, v0
v_and_b32 v25, 15, v25
v_mul_u32_u24 v25, 0xa0, v25
s_mul_i32 s45, s34, 0x3c00
v_add3_u32 v24, v25, v28, s45
s_cmp_eq_u32 s98, 0
s_cselect_b32 s43, 0, 0x80000000
v_or_b32 v2, s43, v2
s_mov_b32 s44, 0x8000
buffer_store_dwordx2 v[4:5], v2, s[4:7], s44 offen sc1
s_waitcnt lgkmcnt(13)
v_pk_add_f32 v[32:33], v[32:33], v[52:53]
v_pk_add_f32 v[34:35], v[34:35], v[54:55]
s_waitcnt lgkmcnt(12)
v_pk_add_f32 v[172:173], v[172:173], v[72:73]
v_pk_add_f32 v[174:175], v[174:175], v[74:75]
v_pk_add_f32 v[172:173], v[172:173], v[32:33]
v_pk_add_f32 v[174:175], v[174:175], v[34:35]
v_cvt_pk_bf16_f32 v172, v172, v173
v_cvt_pk_bf16_f32 v173, v174, v175
s_waitcnt lgkmcnt(10)
v_pk_add_f32 v[36:37], v[36:37], v[56:57]
v_pk_add_f32 v[38:39], v[38:39], v[58:59]
s_waitcnt lgkmcnt(9)
v_pk_add_f32 v[176:177], v[176:177], v[76:77]
v_pk_add_f32 v[178:179], v[178:179], v[78:79]
v_pk_add_f32 v[176:177], v[176:177], v[36:37]
v_pk_add_f32 v[178:179], v[178:179], v[38:39]
v_cvt_pk_bf16_f32 v176, v176, v177
v_cvt_pk_bf16_f32 v177, v178, v179
s_waitcnt lgkmcnt(7)
v_pk_add_f32 v[40:41], v[40:41], v[60:61]
v_pk_add_f32 v[42:43], v[42:43], v[62:63]
s_waitcnt lgkmcnt(6)
v_pk_add_f32 v[180:181], v[180:181], v[80:81]
v_pk_add_f32 v[182:183], v[182:183], v[82:83]
v_pk_add_f32 v[180:181], v[180:181], v[40:41]
v_pk_add_f32 v[182:183], v[182:183], v[42:43]
v_cvt_pk_bf16_f32 v180, v180, v181
v_cvt_pk_bf16_f32 v181, v182, v183
s_waitcnt lgkmcnt(4)
v_pk_add_f32 v[44:45], v[44:45], v[64:65]
v_pk_add_f32 v[46:47], v[46:47], v[66:67]
s_waitcnt lgkmcnt(3)
v_pk_add_f32 v[184:185], v[184:185], v[84:85]
v_pk_add_f32 v[186:187], v[186:187], v[86:87]
v_pk_add_f32 v[184:185], v[184:185], v[44:45]
v_pk_add_f32 v[186:187], v[186:187], v[46:47]
v_cvt_pk_bf16_f32 v184, v184, v185
v_cvt_pk_bf16_f32 v185, v186, v187
s_waitcnt lgkmcnt(1)
v_pk_add_f32 v[48:49], v[48:49], v[68:69]
v_pk_add_f32 v[50:51], v[50:51], v[70:71]
s_waitcnt lgkmcnt(0)
v_pk_add_f32 v[188:189], v[188:189], v[88:89]
v_pk_add_f32 v[190:191], v[190:191], v[90:91]
v_pk_add_f32 v[188:189], v[188:189], v[48:49]
v_pk_add_f32 v[190:191], v[190:191], v[50:51]
v_cvt_pk_bf16_f32 v188, v188, v189
v_cvt_pk_bf16_f32 v189, v190, v191
ds_write_b64 v24, v[172:173]
ds_write_b64 v24, v[176:177] offset:32
ds_write_b64 v24, v[180:181] offset:64
ds_write_b64 v24, v[184:185] offset:96
ds_write_b64 v24, v[188:189] offset:128
ds_read_b128 v[32:35], v27
ds_read_b128 v[36:39], v27 offset:1024
ds_read_b128 v[40:43], v27 offset:2048
s_waitcnt lgkmcnt(2)
buffer_store_dwordx4 v[32:35], v6, s[12:15], 0 offen
s_waitcnt lgkmcnt(1)
buffer_store_dwordx4 v[36:39], v7, s[12:15], 0 offen
s_waitcnt lgkmcnt(0)
buffer_store_dwordx4 v[40:43], v8, s[12:15], 0 offen
s_nop 1
s_endpgm
.Lk_fws_1:
s_mov_b32 s42, 0x10000
.Lk_fwl_3:
buffer_load_dwordx2 v[226:227], v3, s[4:7], 0 offen sc1
s_waitcnt vmcnt(0)
v_cmp_eq_u64_e64 s[94:95], s[40:41], v[226:227]
s_nop 0
s_cmp_eq_u64 s[94:95], exec
s_cbranch_scc1 .Lk_fwb_2
s_sub_u32 s42, s42, 1
s_cbranch_scc0 .Lk_fwl_3
s_branch .Lk_fwb_2
.Lk_fls_4:
s_mov_b32 s42, 0x10000
.Lk_fll_6:
buffer_load_dwordx2 v[226:227], v3, s[4:7], s47 offen sc1
s_waitcnt vmcnt(0)
)ASM"
R"ASM(
v_cmp_eq_u64_e64 s[94:95], s[40:41], v[226:227]
s_nop 0
s_cmp_eq_u64 s[94:95], exec
s_cbranch_scc1 .Lk_flb_5
s_sub_u32 s42, s42, 1
s_cbranch_scc0 .Lk_fll_6
s_branch .Lk_flb_5
.Lk_g1s_13:
s_mov_b32 s42, 0x10000
.Lk_g1l_15:
buffer_load_dwordx2 v[58:59], v3, s[4:7], s86 offen sc1
s_waitcnt vmcnt(0)
v_cmp_eq_u64_e64 s[94:95], s[40:41], v[58:59]
s_nop 0
s_cmp_eq_u64 s[94:95], exec
s_cbranch_scc1 .Lk_g1b_14
s_sub_u32 s42, s42, 1
s_cbranch_scc0 .Lk_g1l_15
s_branch .Lk_g1b_14
.Lk_g2s_16:
s_mov_b32 s42, 0x10000
.Lk_g2l_18:
buffer_load_dwordx2 v[218:219], v3, s[4:7], s86 offen sc1
s_waitcnt vmcnt(0)
v_cmp_eq_u64_e64 s[94:95], s[40:41], v[218:219]
s_nop 0
s_cmp_eq_u64 s[94:95], exec
s_cbranch_scc1 .Lk_g2b_17
s_sub_u32 s42, s42, 1
s_cbranch_scc0 .Lk_g2l_18
s_branch .Lk_g2b_17
.Lk_g3s_19:
s_mov_b32 s42, 0x10000
.Lk_g3l_21:
buffer_load_dwordx2 v[218:219], v3, s[4:7], s86 offen sc1
s_waitcnt vmcnt(0)
v_cmp_eq_u64_e64 s[94:95], s[40:41], v[218:219]
s_nop 0
s_cmp_eq_u64 s[94:95], exec
s_cbranch_scc1 .Lk_g3b_20
s_sub_u32 s42, s42, 1
s_cbranch_scc0 .Lk_g3l_21
s_branch .Lk_g3b_20
.Lk_end:
)ASM"
      : "+v"(tid)
      : "s"(__builtin_amdgcn_kernarg_segment_ptr()), "s"(blockIdx.x)
      : "v1", "v2", "v3", "v4", "v5", "v6", "v7", "v8", "v9", "v10", "v11", "v12", "v13", "v14", "v15", "v16", "v17", "v18", "v19", "v20", "v21", "v22", "v23", "v24", "v25", "v26", "v27", "v28", "v29", "v30", "v31", "v32", "v33", "v34", "v35", "v36", "v37", "v38", "v39", "v40", "v41", "v42", "v43", "v44", "v45", "v46", "v47", "v48", "v49", "v50", "v51", "v52", "v53", "v54", "v55", "v56", "v57", "v58", "v59", "v60", "v61", "v62", "v63", "v64", "v65", "v66", "v67", "v68", "v69", "v70", "v71", "v72", "v73", "v74", "v75", "v76", "v77", "v78", "v79", "v80", "v81", "v82", "v83", "v84", "v85", "v86", "v87", "v88", "v89", "v90", "v91", "v92", "v93", "v94", "v95", "v96", "v97", "v98", "v99", "v100", "v101", "v102", "v103", "v104", "v105", "v106", "v107", "v108", "v109", "v110", "v111", "v112", "v113", "v114", "v115", "v116", "v117", "v118", "v119", "v120", "v121", "v122", "v123", "v124", "v125", "v126", "v127", "v128", "v129", "v130", "v131", "v132", "v133", "v134", "v135", "v136", "v137", "v138", "v139", "v140", "v141", "v142", "v143", "v144", "v145", "v146", "v147", "v148", "v149", "v150", "v151", "v152", "v153", "v154", "v155", "v156", "v157", "v158", "v159", "v160", "v161", "v162", "v163", "v164", "v165", "v166", "v167", "v168", "v169", "v170", "v171", "v172", "v173", "v174", "v175", "v176", "v177", "v178", "v179", "v180", "v181", "v182", "v183", "v184", "v185", "v186", "v187", "v188", "v189", "v190", "v191", "v192", "v193", "v194", "v195", "v196", "v197", "v198", "v199", "v200", "v201", "v202", "v203", "v204", "v205", "v206", "v207", "v208", "v209", "v210", "v211", "v212", "v213", "v214", "v215", "v216", "v217", "v218", "v219", "v220", "v221", "v222", "v223", "v224", "v225", "v226", "v227", "v228", "v229", "v230", "v231", "v232", "v233", "v234", "v235", "v236", "v237", "v238", "v239", "v240", "v241", "v242", "v243", "v244", "v245", "v246", "v247", "v248", "v249", "v250", "v251", "v252", "v253", "v254", "v255", "a0", "a1", "a2", "a3", "a4", "a5", "a6", "a7", "a8", "a9", "a10", "a11", "a12", "a13", "a14", "a15", "a16", "a17", "a18", "a19", "a20", "a21", "a22", "a23", "a24", "a25", "a26", "a27", "a28", "a29", "a30", "a31", "a32", "a33", "a34", "a35", "a36", "a37", "a38", "a39", "a40", "a41", "a42", "a43", "a44", "a45", "a46", "a47", "a48", "a49", "a50", "a51", "a52", "a53", "a54", "a55", "a56", "a57", "a58", "a59", "a60", "a61", "a62", "a63", "a64", "a65", "a66", "a67", "a68", "a69", "a70", "a71", "a72", "a73", "a74", "a75", "a76", "a77", "a78", "a79", "a80", "a81", "a82", "a83", "a84", "a85", "a86", "a87", "a88", "a89", "a90", "a91", "a92", "a93", "a94", "a95", "a96", "a97", "a98", "a99", "a100", "a101", "a102", "a103", "a104", "a105", "a106", "a107", "a108", "a109", "a110", "a111", "a112", "a113", "a114", "a115", "a116", "a117", "a118", "a119", "a120", "a121", "a122", "a123", "a124", "a125", "a126", "a127", "a128", "a129", "a130", "a131", "a132", "a133", "a134", "a135", "a136", "a137", "a138", "a139", "a140", "a141", "a142", "a143", "a144", "a145", "a146", "a147", "a148", "a149", "a150", "a151", "a152", "a153", "a154", "a155", "a156", "a157", "a158", "a159", "a160", "a161", "a162", "a163", "a164", "a165", "a166", "a167", "a168", "a169", "a170", "a171", "a172", "a173", "a174", "a175", "a176", "a177", "a178", "a179", "a180", "a181", "a182", "a183", "a184", "a185", "a186", "a187", "a188", "a189", "a190", "a191", "a192", "a193", "a194", "a195", "a196", "a197", "a198", "a199", "a200", "a201", "a202", "a203", "a204", "a205", "a206", "a207", "a208", "a209", "a210", "a211", "a212", "a213", "a214", "a215", "a216", "a217", "a218", "a219", "a220", "a221", "a222", "a223", "a224", "a225", "a226", "a227", "a228", "a229", "a230", "a231", "a232", "a233", "a234", "a235", "a236", "a237", "a238", "a239", "a240", "a241", "a242", "a243", "a244", "a245", "a246", "a247", "a248", "a249", "a250", "a251", "a252", "a253", "a254", "a255", "s4", "s5", "s6", "s7", "s8", "s9", "s10", "s11", "s12", "s13", "s14", "s15", "s16", "s17", "s18", "s19", "s20", "s21", "s22", "s23", "s24", "s25", "s26", "s27", "s28", "s29", "s30", "s31", "s34", "s35", "s36", "s37", "s38", "s39", "s40", "s41", "s42", "s43", "s44", "s45", "s46", "s47", "s48", "s49", "s50", "s51", "s52", "s53", "s54", "s55", "s56", "s57", "s58", "s59", "s60", "s61", "s62", "s63", "s64", "s65", "s66", "s67", "s68", "s69", "s70", "s71", "s72", "s73", "s74", "s75", "s76", "s77", "s78", "s79", "s80", "s81", "s82", "s83", "s84", "s85", "s86", "s87", "s88", "s89", "s90", "s91", "s92", "s93", "s94", "s95", "s96", "s97", "s98", "s99", "vcc", "scc", "memory");
#endif
}

extern "C" void kernel_launch(void* const* d_in, const int* in_sizes, int n_in, void* d_out, int out_size, void* d_ws,
                              size_t ws_size, hipStream_t stream) {
  (void)in_sizes; (void)n_in; (void)out_size; (void)ws_size;
  (void)hipFuncSetAttribute((const void*)block_fwd, hipFuncAttributeMaxDynamicSharedMemorySize, 163840);
  hipLaunchKernelGGL(block_fwd, dim3(256), dim3(256), 163840, stream, d_in[0], d_in[1], d_in[2], d_in[4], d_in[5],
                     d_in[6], d_in[7], d_out, d_ws);
}
```
